```python
import torch

def _softmax(x, axis=-1):
    return torch.softmax(x, dim=axis)
def _gelu(x):
    return torch.nn.functional.gelu(x, approximate="tanh")
def _silu(x):
    return torch.nn.functional.silu(x)
def _sigmoid(x):
    return torch.sigmoid(x)
def _layernorm(x, axis=-1, eps=1e-5):
    m = x.mean(dim=axis, keepdim=True)
    v = x.var(dim=axis, keepdim=True, unbiased=False)
    return (x - m) / torch.sqrt(v + eps)
def _scatter_add(tgt, idx, src, axis=0):
    return tgt.clone().index_add_(axis, idx, src)
def _segment_sum(src, seg, n):
    out = torch.zeros((n,) + src.shape[1:], dtype=src.dtype)
    return out.index_add_(0, seg, src)
def _pad(x, pw):
    flat = tuple(v for pair in reversed(pw) for v in pair)
    return torch.nn.functional.pad(x, flat)

kernel_name = 'gated_mix__compressed_kv_cla__dense_activation_relu__prompt'

def setup_inputs():

    _w = (lambda a: (lambda t: t.to(torch.bfloat16) if t.is_floating_point() else t)(torch.as_tensor(a)))
    def _A_setup():
        B, H, Sq, Sk, D, M = 1, 32, 2048, 2048, 128, 576
        Dc = 4 * D
        return {
            "x": _w((lambda *s: torch.randn(*s, dtype=torch.bfloat16) * 0.05)(B, Sq, M)),
            "c_kv": _w((lambda *s: torch.randn(*s, dtype=torch.bfloat16) * 0.8)(B, Sk, Dc)),
            "w_q": _w((lambda *s: torch.randn(*s, dtype=torch.bfloat16) * 6.4)(M, H, D) * (M ** -0.5)),
            "w_uk": _w((lambda *s: torch.randn(*s, dtype=torch.bfloat16) * 6.4)(Dc, H, D) * (Dc ** -0.5)),
            "w_uv": _w((lambda *s: torch.randn(*s, dtype=torch.bfloat16) * 0.8)(Dc, H, D) * (Dc ** -0.5)),
            "w_o": _w((lambda *s: torch.randn(*s, dtype=torch.bfloat16) * 0.8)(H, D, M) * ((H * D) ** -0.5)),
        }
    _A_in = _A_setup()

    def _B_setup():
        B, S, D, Dff = 1, 2048, 576, 4608
        return {
            "x": _w((lambda *s: torch.randn(*s, dtype=torch.bfloat16) * 0.05)(B, S, D)),
            "w1": _w((lambda *s: torch.randn(*s, dtype=torch.bfloat16) * 0.05)(D, Dff)),
            "w2": _w((lambda *s: torch.randn(*s, dtype=torch.bfloat16) * 0.05)(Dff, D)),
        }
    _B_in = _B_setup()

    return {
        "A_x": _A_in["x"],
        "A_c_kv": _A_in["c_kv"],
        "A_w_q": _A_in["w_q"],
        "A_w_uk": _A_in["w_uk"],
        "A_w_uv": _A_in["w_uv"],
        "A_w_o": _A_in["w_o"],
        "B_x": _B_in["x"],
        "B_w1": _B_in["w1"],
        "B_w2": _B_in["w2"],
        "gate_w": _w((lambda *s: torch.randn(*s, dtype=torch.bfloat16) * 0.05)(576) * 16.0),
    }

def reference(A_x, A_c_kv, A_w_q, A_w_uk, A_w_uv, A_w_o, B_x, B_w1, B_w2, gate_w):
    def _A_ref(x, c_kv, w_q, w_uk, w_uv, w_o):
        B, Sq, M = x.shape[0], x.shape[1], x.shape[2]
        Sk = c_kv.shape[1]
        H, D = w_q.shape[1], w_q.shape[2]
        q = torch.einsum("bqm,mhd->bhqd", x, w_q)
        k = torch.einsum("bkc,chd->bhkd", c_kv, w_uk)
        v = torch.einsum("bkc,chd->bhkd", c_kv, w_uv)
        scores = torch.einsum("bhqd,bhkd->bhqk", q, k) * (D ** -0.5)
        off = Sk - Sq
        qi = torch.arange(Sq)[:, None]
        kj = torch.arange(Sk)[None, :]
        cmask = kj <= (qi + off)
        scores = torch.where(cmask[None, None], scores, -1e9)
        p = _softmax(scores, axis=-1)
        ctx = torch.einsum("bhqk,bhkd->bhqd", p, v)
        return torch.einsum("bhqd,hdm->bqm", ctx, w_o)

    def _B_ref(x, w1, w2):
        h = torch.einsum("bsd,df->bsf", x, w1)
        zero = torch.zeros_like(h)
        h = torch.maximum(h, zero)
        return torch.einsum("bsf,fd->bsd", h, w2)

    x = A_x
    g = _sigmoid((x * gate_w).sum(-1, keepdims=True))
    a = _A_ref(x, A_c_kv, A_w_q, A_w_uk, A_w_uv, A_w_o)
    b = _B_ref(x, B_w1, B_w2)
    return g * a + (1 - g) * b
```

```cpp
#include <hip/hip_runtime.h>
#include <hip/hip_bf16.h>
#include <cstdint>

typedef short bf16x8 __attribute__((ext_vector_type(8)));
typedef short s16x4 __attribute__((ext_vector_type(4)));
typedef float f32x16 __attribute__((ext_vector_type(16)));
typedef float f32x4 __attribute__((ext_vector_type(4)));
typedef unsigned u32x4 __attribute__((ext_vector_type(4)));
typedef unsigned u32x2 __attribute__((ext_vector_type(2)));
typedef unsigned short u16;

constexpr int S_ = 2048, DM = 576, DC = 512, NH = 32, HD = 128, NQ = 4096, DFF = 4608;

__device__ __forceinline__ unsigned cvtpk(float lo, float hi) {
    unsigned r; asm volatile("v_cvt_pk_bf16_f32 %0, %1, %2" : "=v"(r) : "v"(lo), "v"(hi)); return r;
}
__device__ __forceinline__ int crow(int r, int hi) { return (r & 3) + 8 * (r >> 2) + 4 * hi; }
__device__ __forceinline__ int v_rd_base(int lane) { return ((lane & 3) << 3) | (((lane >> 2) & 3) << 6) | (((lane >> 4) & 1) << 5) | (((lane >> 5) & 1) << 8); }
#define SBAR() __builtin_amdgcn_sched_barrier(0)

__device__ __forceinline__ void glds16(const void* g, void* l) {
    __builtin_amdgcn_global_load_lds((const __attribute__((address_space(1))) void*)g, (__attribute__((address_space(3))) void*)l, 16, 0, 0);
}

template <int BM, int BN, int WM, int WN, bool SWAP>
struct Gemm {
    static constexpr int BK = 64;
    static constexpr int TM = BM / WM / 32, TN = BN / WN / 32;
    static constexpr int A_BYTES = BM * BK * 2, B_BYTES = BN * BK * 2, STG = A_BYTES + B_BYTES;
    static constexpr int NA = BM * 8 / 512, NB = BN * 8 / 512, CB = BN / 32;
    __device__ static __forceinline__ void stage(const u16* A, int lda, const u16* B, int ldb, int k0, char* buf, int tid) {
        const int w = tid >> 6, lane = tid & 63;
#pragma unroll
        for (int i = 0; i < NA; ++i) { const int c = (i * 8 + w) * 64 + lane, row = c >> 3, sl = c & 7, sc = sl ^ ((row >> 1) & 7);
            glds16(A + (size_t)row * lda + k0 + sc * 8, buf + (i * 8 + w) * 1024); }
#pragma unroll
        for (int i = 0; i < NB; ++i) { const int c = (i * 8 + w) * 64 + lane, s = c >> 5, rk = (c >> 2) & 7, cc = c & 3;
            const int kh = s / CB, cb = s - kh * CB, kk = kh * 8 + rk, k = (kk & ~0xC) | ((kk & 4) << 1) | ((kk & 8) >> 1);
            glds16(B + (size_t)(k0 + k) * ldb + cb * 32 + cc * 8, buf + A_BYTES + (i * 8 + w) * 1024); }
    }
    __device__ static __forceinline__ void compute(const char* buf, int wm, int wn, int lane, f32x16 (&acc)[TM][TN]) {
        const int r32 = lane & 31, hi = lane >> 5;
        const int bb = (int)(uintptr_t)(buf + A_BYTES) + v_rd_base(lane) + wn * TN * 512;
#pragma unroll
        for (int ks = 0; ks < 4; ++ks) {
            bf16x8 af[TM]; s16x4 bl[TN], bh[TN];
#pragma unroll
            for (int t = 0; t < TN; ++t) {
                asm volatile("ds_read_b64_tr_b16 %0, %1 offset:%2" : "=&v"(bl[t]) : "v"(bb), "i"((ks * 2 * CB + t) * 512) : "memory");
                asm volatile("ds_read_b64_tr_b16 %0, %1 offset:%2" : "=&v"(bh[t]) : "v"(bb), "i"(((ks * 2 + 1) * CB + t) * 512) : "memory"); }
#pragma unroll
            for (int t = 0; t < TM; ++t) { const int row = (wm * TM + t) * 32 + r32;
                af[t] = *(const bf16x8*)(buf + row * 128 + (((ks * 2 + hi) ^ ((row >> 1) & 7)) << 4)); }
            asm volatile("s_waitcnt lgkmcnt(0)" ::: "memory"); SBAR();
#pragma unroll
            for (int tn = 0; tn < TN; ++tn) { const bf16x8 bf = {bl[tn][0], bl[tn][1], bl[tn][2], bl[tn][3], bh[tn][0], bh[tn][1], bh[tn][2], bh[tn][3]};
#pragma unroll
                for (int tm = 0; tm < TM; ++tm) {
                    if (SWAP) acc[tm][tn] = __builtin_amdgcn_mfma_f32_32x32x16_bf16(bf, af[tm], acc[tm][tn], 0, 0, 0);
                    else      acc[tm][tn] = __builtin_amdgcn_mfma_f32_32x32x16_bf16(af[tm], bf, acc[tm][tn], 0, 0, 0); } }
        }
    }
    __device__ static __forceinline__ void run(const u16* A, int lda, const u16* B, int ldb, int k0, int nst, char* lds, f32x16 (&acc)[TM][TN]) {
        const int tid = threadIdx.x, w = __builtin_amdgcn_readfirstlane(tid >> 6), lane = tid & 63, wm = w / WN, wn = w % WN;
#pragma unroll
        for (int i = 0; i < TM; ++i)
#pragma unroll
            for (int j = 0; j < TN; ++j) acc[i][j] = f32x16{};
        stage(A, lda, B, ldb, k0, lds, tid);
        asm volatile("s_waitcnt vmcnt(0)" ::: "memory"); __builtin_amdgcn_s_barrier();
        for (int t = 0; t < nst; ++t) {
            char* cur = lds + (t & 1) * STG; char* nxt = lds + ((t + 1) & 1) * STG;
            if (t + 1 < nst) stage(A, lda, B, ldb, k0 + (t + 1) * 64, nxt, tid);
            compute(cur, wm, wn, lane, acc);
            asm volatile("s_waitcnt vmcnt(0) lgkmcnt(0)" ::: "memory"); __builtin_amdgcn_s_barrier();
        }
    }
};

constexpr int G1_LDS = 8 * 128 * 144;
__global__ __launch_bounds__(512, 1) void k1_proj(const u16* __restrict__ x, const u16* __restrict__ ckv, const u16* __restrict__ wq,
        const u16* __restrict__ wuk, const u16* __restrict__ wuv, const u16* __restrict__ w1, u16* __restrict__ qkv, u16* __restrict__ hbuf) {
    extern __shared__ __attribute__((aligned(16))) char lds[];
    using G = Gemm<256, 256, 2, 4, true>;
    int b = blockIdx.x; const int mt = b & 7; int nt = b >> 3;
    const u16* A; const u16* B; u16* C; int lda, ldb, ldc, nst; bool relu = false;
    if (nt < 16) { A = x; lda = DM; B = wq; ldb = NQ; C = qkv; ldc = NQ; nst = 9; }
    else if (nt < 32) { nt -= 16; A = ckv; lda = DC; B = wuk; ldb = NQ; C = qkv + (size_t)S_ * NQ; ldc = NQ; nst = 8; }
    else if (nt < 48) { nt -= 32; A = ckv; lda = DC; B = wuv; ldb = NQ; C = qkv + (size_t)2 * S_ * NQ; ldc = NQ; nst = 8; }
    else { nt -= 48; A = x; lda = DM; B = w1; ldb = DFF; C = hbuf; ldc = DFF; nst = 9; relu = true; }
    f32x16 acc[G::TM][G::TN];
    G::run(A + (size_t)mt * 256 * lda, lda, B + nt * 256, ldb, 0, nst, lds, acc);
    const int tid = threadIdx.x, w = tid >> 6, lane = tid & 63, r32 = lane & 31, hi = lane >> 5, wm = w >> 2, wn = w & 3;
    char* ep = lds + w * (128 * 144);
#pragma unroll
    for (int tm = 0; tm < 4; ++tm)
#pragma unroll
        for (int tn = 0; tn < 2; ++tn)
#pragma unroll
            for (int q = 0; q < 4; ++q) { f32x16& a = acc[tm][tn]; float v0 = a[4 * q], v1 = a[4 * q + 1], v2 = a[4 * q + 2], v3 = a[4 * q + 3];
                if (relu) { v0 = fmaxf(v0, 0.f); v1 = fmaxf(v1, 0.f); v2 = fmaxf(v2, 0.f); v3 = fmaxf(v3, 0.f); }
                u32x2 pk = {cvtpk(v0, v1), cvtpk(v2, v3)};
                *(u32x2*)(ep + (tm * 32 + r32) * 144 + (tn * 32 + 8 * q + 4 * hi) * 2) = pk; }
    asm volatile("s_waitcnt lgkmcnt(0)" ::: "memory");
    u16* Cw = C + (size_t)(mt * 256 + wm * 128) * ldc + nt * 256 + wn * 64;
#pragma unroll
    for (int i = 0; i < 16; ++i) { const int row = i * 8 + (lane >> 3), ch = lane & 7;
        const u32x4 v = *(const u32x4*)(ep + row * 144 + ch * 16);
        *(u32x4*)(Cw + (size_t)row * ldc + ch * 8) = v; }
}

constexpr int G3_LDS = 2 * (256 * 128 + 192 * 128);
__global__ __launch_bounds__(512, 1) void k3_out(const u16* __restrict__ ctx, const u16* __restrict__ wo, const u16* __restrict__ hbuf,
        const u16* __restrict__ w2, float* __restrict__ part) {
    extern __shared__ __attribute__((aligned(16))) char lds[];
    using G = Gemm<256, 192, 4, 2, false>;
    const int b = blockIdx.x, tile = b % 24, sp = b / 24, mt = tile & 7, nt = tile >> 3;
    const u16* A; const u16* B; int lda, k0, nst;
    if (sp < 5) { A = ctx; lda = NQ; B = wo; const int s0 = (sp * 64) / 5, s1 = ((sp + 1) * 64) / 5; k0 = s0 * 64; nst = s1 - s0; }
    else { const int p = sp - 5; A = hbuf; lda = DFF; B = w2; const int s0 = (p * 72) / 5, s1 = ((p + 1) * 72) / 5; k0 = s0 * 64; nst = s1 - s0; }
    f32x16 acc[G::TM][G::TN];
    G::run(A + (size_t)mt * 256 * lda, lda, B + nt * 192, DM, k0, nst, lds, acc);
    const int tid = threadIdx.x, w = tid >> 6, lane = tid & 63, r32 = lane & 31, hi = lane >> 5, wm = w >> 1, wn = w & 1;
    float* P = part + (size_t)sp * S_ * DM + (size_t)(mt * 256 + wm * 64) * DM + nt * 192 + wn * 96 + r32;
#pragma unroll
    for (int tm = 0; tm < 2; ++tm)
#pragma unroll
        for (int tn = 0; tn < 3; ++tn)
#pragma unroll
            for (int r = 0; r < 16; ++r) P[(size_t)(tm * 32 + crow(r, hi)) * DM + tn * 32] = acc[tm][tn][r];
}

__device__ __forceinline__ float bfr(float v) { return __uint_as_float(cvtpk(v, v) << 16); }
__device__ __forceinline__ float bf2f(u16 v) { return __uint_as_float((unsigned)v << 16); }
__global__ __launch_bounds__(256) void k4_final(const float* __restrict__ part, const u16* __restrict__ x, const u16* __restrict__ gw, u16* __restrict__ out) {
    const int wv = threadIdx.x >> 6, lane = threadIdx.x & 63, row = blockIdx.x * 4 + wv;
    float s = 0.f;
    for (int i = lane; i < DM; i += 64) s += bfr(bf2f(x[(size_t)row * DM + i]) * bf2f(gw[i]));
    for (int o = 32; o > 0; o >>= 1) s += __shfl_xor(s, o);
    s = bfr(s);
    const float g = bfr(1.f / (1.f + __expf(-s))), g1 = bfr(1.f - g);
    for (int c = lane * 4; c < DM; c += 256) {
        f32x4 a = {0, 0, 0, 0}, bsum = {0, 0, 0, 0};
#pragma unroll
        for (int p = 0; p < 5; ++p) { a += *(const f32x4*)(part + ((size_t)p * S_ + row) * DM + c); bsum += *(const f32x4*)(part + ((size_t)(p + 5) * S_ + row) * DM + c); }
        float o4[4];
#pragma unroll
        for (int j = 0; j < 4; ++j) o4[j] = bfr(g * bfr(a[j])) + bfr(g1 * bfr(bsum[j]));
        u32x2 pk = {cvtpk(o4[0], o4[1]), cvtpk(o4[2], o4[3])};
        *(u32x2*)(out + (size_t)row * DM + c) = pk;
    }
}

constexpr float SCALE = 0.08838834764831845f;
constexpr int AT_BUF = 65536;
constexpr int AT_LDS = 2 * AT_BUF + 8 * 256;
__device__ __forceinline__ void attn_stage(const u16* Kh, const u16* Vh, int key0, char* buf, int tid) {
    const int w = tid >> 6, lane = tid & 63;
    int ko = (w * 4 + (lane >> 4)) * NQ + (((lane & 15) ^ ((lane >> 4) & 3) ^ ((w & 1) << 2)) << 3);
    const int kk = (w >> 1) * 8 + ((lane >> 2) & 7), k = (kk & ~0xC) | ((kk & 4) << 1) | ((kk & 8) >> 1);
    int vo = k * NQ + ((w & 1) * 2 + (lane >> 5)) * 32 + (lane & 3) * 8;
    asm volatile("" : "+v"(ko), "+v"(vo));
    const u16* kp = Kh + (size_t)key0 * NQ; const u16* vp = Vh + (size_t)key0 * NQ;
#pragma unroll
    for (int i = 0; i < 4; ++i) { glds16(kp + (size_t)i * 32 * NQ + ko, buf + (i * 8 + w) * 1024); glds16(vp + (size_t)i * 32 * NQ + vo, buf + 32768 + (i * 8 + w) * 1024); }
}
__global__ __launch_bounds__(512, 1) void k2_attn(const u16* __restrict__ qkv, u16* __restrict__ ctx) {
    extern __shared__ __attribute__((aligned(16))) char lds[];
    const int tid = threadIdx.x, w = __builtin_amdgcn_readfirstlane(tid >> 6), lane = tid & 63, r32 = lane & 31, hi = lane >> 5;
    const int grp = w >> 2, wq = w & 3;
    const int head = blockIdx.x >> 3, pr = blockIdx.x & 7;
    const u16* Qh = qkv + head * HD; const u16* Kh = qkv + (size_t)S_ * NQ + head * HD; const u16* Vh = qkv + (size_t)2 * S_ * NQ + head * HD;
    float* sm = (float*)(lds + 2 * AT_BUF) + w * 64;
    constexpr float C2 = 1.4426950408889634f * SCALE;
    for (int pass = 0; pass < 2; ++pass) {
        const int xb = pass == 0 ? 15 - pr : pr, q0 = xb * 128 + wq * 32, nit = xb + 1;
        bf16x8 qr[8];
#pragma unroll
        for (int d0 = 0; d0 < 8; ++d0) qr[d0] = *(const bf16x8*)(Qh + (size_t)(q0 + r32) * NQ + d0 * 16 + hi * 8);
        float m_reg = -1e30f, l_reg = 0.f; f32x16 o[4] = {};
        attn_stage(Kh, Vh, 0, lds, tid);
        asm volatile("s_waitcnt vmcnt(0)" ::: "memory"); __builtin_amdgcn_s_barrier();
        for (int it = 0; it < nit; ++it) {
            const char* cur = lds + (it & 1) * AT_BUF;
            if (it + 1 < nit) attn_stage(Kh, Vh, (it + 1) * 128, lds + ((it + 1) & 1) * AT_BUF, tid);
            const char* Kt = cur + grp * 16384; const int vb0 = (int)(uintptr_t)(cur + 32768 + grp * 16384) + v_rd_base(lane);
            f32x16 p0 = {}, p1 = {};
#pragma unroll
            for (int dd = 0; dd < 4; ++dd) { const char* a = Kt + r32 * 256 + (((dd * 2 + hi) ^ (r32 & 7)) << 4);
#pragma unroll
                for (int e = 0; e < 2; ++e) {
                const bf16x8 b0 = *(const bf16x8*)(a + e * 128), b1 = *(const bf16x8*)(a + e * 128 + 8192);
                p0 = __builtin_amdgcn_mfma_f32_32x32x16_bf16(b0, qr[dd + 4 * e], p0, 0, 0, 0);
                p1 = __builtin_amdgcn_mfma_f32_32x32x16_bf16(b1, qr[dd + 4 * e], p1, 0, 0, 0); } }
            const int kb = (2 * it + grp) * 64;
            if (kb + 63 > q0) { const int dq = q0 + r32 - 4 * hi - kb; const float NEG = -__builtin_inff();
#pragma unroll
                for (int r = 0; r < 16; ++r) { const int c = (r & 3) + 8 * (r >> 2); if (dq - c < 0) p0[r] = NEG; if (dq - c - 32 < 0) p1[r] = NEG; } }
            float pmax = p0[0];
#pragma unroll
            for (int r = 1; r < 16; ++r) pmax = fmaxf(pmax, p0[r]);
#pragma unroll
            for (int r = 0; r < 16; ++r) pmax = fmaxf(pmax, p1[r]);
            { auto rr = __builtin_amdgcn_permlane32_swap(__float_as_uint(pmax), __float_as_uint(pmax), false, false);
              pmax = fmaxf(__uint_as_float(rr[0]), __uint_as_float(rr[1])); }
            const float mn = fmaxf(m_reg, pmax), alpha = __builtin_amdgcn_exp2f((m_reg - mn) * C2); m_reg = mn;
            const float mnL = -mn * C2; float ps = 0.f;
#pragma unroll
            for (int r = 0; r < 16; ++r) { p0[r] = __builtin_amdgcn_exp2f(fmaf(p0[r], C2, mnL)); p1[r] = __builtin_amdgcn_exp2f(fmaf(p1[r], C2, mnL)); ps += p0[r] + p1[r]; }
            { auto rr = __builtin_amdgcn_permlane32_swap(__float_as_uint(ps), __float_as_uint(ps), false, false);
              ps = __uint_as_float(rr[0]) + __uint_as_float(rr[1]); }
            l_reg = l_reg * alpha + ps;
            if (__any(alpha < 1.f)) { if (hi == 0) sm[r32] = alpha; asm volatile("s_waitcnt lgkmcnt(0)" ::: "memory");
#pragma unroll
                for (int d_ = 0; d_ < 4; ++d_)
#pragma unroll
                    for (int r = 0; r < 16; ++r) o[d_][r] *= sm[crow(r, hi)]; }
            bf16x8 pa[4];
#define PK4(P, B_, OUT) do { unsigned a0 = cvtpk(P[B_+0], P[B_+1]), a1 = cvtpk(P[B_+2], P[B_+3]), b0 = cvtpk(P[B_+4], P[B_+5]), b1 = cvtpk(P[B_+6], P[B_+7]); \
        auto r0 = __builtin_amdgcn_permlane32_swap(a0, b0, false, false); auto r1 = __builtin_amdgcn_permlane32_swap(a1, b1, false, false); \
        u32x4 ww = {r0[0], r1[0], r0[1], r1[1]}; OUT = *reinterpret_cast<bf16x8*>(&ww); } while (0)
            PK4(p0, 0, pa[0]); PK4(p0, 8, pa[1]); PK4(p1, 0, pa[2]); PK4(p1, 8, pa[3]);
#undef PK4
#pragma unroll
            for (int dh = 0; dh < 8; ++dh) { const int d0 = dh >> 1, k2 = (dh & 1) * 2; s16x4 lo[4], hh[4];
#pragma unroll
                for (int ks = k2; ks < k2 + 2; ++ks) {
                    asm volatile("ds_read_b64_tr_b16 %0, %1 offset:%2" : "=&v"(lo[ks]) : "v"(vb0), "i"(d0 * 512 + ks * 4096) : "memory");
                    asm volatile("ds_read_b64_tr_b16 %0, %1 offset:%2" : "=&v"(hh[ks]) : "v"(vb0), "i"(d0 * 512 + ks * 4096 + 2048) : "memory"); }
                asm volatile("s_waitcnt lgkmcnt(0)" ::: "memory"); SBAR();
#pragma unroll
                for (int ks = k2; ks < k2 + 2; ++ks) o[d0] = __builtin_amdgcn_mfma_f32_32x32x16_bf16(pa[ks],
                    (bf16x8){lo[ks][0], lo[ks][1], lo[ks][2], lo[ks][3], hh[ks][0], hh[ks][1], hh[ks][2], hh[ks][3]}, o[d0], 0, 0, 0); }
            asm volatile("s_waitcnt vmcnt(0) lgkmcnt(0)" ::: "memory"); __builtin_amdgcn_s_barrier();
        }
        float* ob = (float*)lds + wq * 4096; float* ml = (float*)(lds + 2 * AT_BUF) + (wq + 4) * 64;
        if (grp == 1) { if (hi == 0) { ml[r32] = m_reg; ml[32 + r32] = l_reg; }
#pragma unroll
            for (int d0 = 0; d0 < 4; ++d0)
#pragma unroll
                for (int r = 0; r < 16; ++r) ob[crow(r, hi) * 128 + d0 * 32 + r32] = o[d0][r]; }
        asm volatile("s_waitcnt lgkmcnt(0)" ::: "memory"); __builtin_amdgcn_s_barrier();
        if (grp == 0) {
            const float m1 = ml[r32], l1 = ml[32 + r32], mn = fmaxf(m_reg, m1);
            const float a0 = __builtin_amdgcn_exp2f((m_reg - mn) * C2), a1 = __builtin_amdgcn_exp2f((m1 - mn) * C2), rl = 1.f / (l_reg * a0 + l1 * a1);
            if (hi == 0) { sm[r32] = a0 * rl; sm[32 + r32] = a1 * rl; } asm volatile("s_waitcnt lgkmcnt(0)" ::: "memory");
            u16* Ow = ctx + (size_t)q0 * NQ + head * HD;
#pragma unroll
            for (int r = 0; r < 16; ++r) { const int orow = crow(r, hi); const float f0 = sm[orow], f1 = sm[32 + orow];
#pragma unroll
                for (int d0 = 0; d0 < 4; ++d0) { const float v = o[d0][r] * f0 + ob[orow * 128 + d0 * 32 + r32] * f1; const float vn = __shfl_xor(v, 1);
                    if ((r32 & 1) == 0) *(unsigned*)(Ow + (size_t)orow * NQ + d0 * 32 + r32) = cvtpk(v, vn); } }
        }
        __syncthreads();
    }
}

extern "C" void kernel_launch(void* const* d_in, const int* in_sizes, int n_in, void* d_out, int out_size, void* d_ws, size_t ws_size, hipStream_t stream) {
    const u16* x = (const u16*)d_in[0]; const u16* ckv = (const u16*)d_in[1]; const u16* wq = (const u16*)d_in[2]; const u16* wuk = (const u16*)d_in[3];
    const u16* wuv = (const u16*)d_in[4]; const u16* wo = (const u16*)d_in[5]; const u16* w1 = (const u16*)d_in[7]; const u16* w2 = (const u16*)d_in[8];
    const u16* gw = (const u16*)d_in[9];
    char* ws = (char*)d_ws;
    u16* qkv = (u16*)ws;
    u16* hbuf = (u16*)(ws + (size_t)48 * 1048576);
    u16* ctx = (u16*)(ws + (size_t)68 * 1048576);
    float* part = (float*)(ws + (size_t)84 * 1048576);
    hipFuncSetAttribute((const void*)k1_proj, hipFuncAttributeMaxDynamicSharedMemorySize, G1_LDS);
    hipFuncSetAttribute((const void*)k2_attn, hipFuncAttributeMaxDynamicSharedMemorySize, AT_LDS);
    hipFuncSetAttribute((const void*)k3_out, hipFuncAttributeMaxDynamicSharedMemorySize, G3_LDS);
    k1_proj<<<528, 512, G1_LDS, stream>>>(x, ckv, wq, wuk, wuv, w1, qkv, hbuf);
    k2_attn<<<256, 512, AT_LDS, stream>>>(qkv, ctx);
    k3_out<<<240, 512, G3_LDS, stream>>>(ctx, wo, hbuf, w2, part);
    k4_final<<<512, 256, 0, stream>>>(part, x, gw, (u16*)d_out);
}
```

```cpp
#include <hip/hip_runtime.h>
#include <hip/hip_bf16.h>
#include <cstdint>

typedef short bf16x8 __attribute__((ext_vector_type(8)));
typedef short s16x4 __attribute__((ext_vector_type(4)));
typedef float f32x16 __attribute__((ext_vector_type(16)));
typedef float f32x4 __attribute__((ext_vector_type(4)));
typedef unsigned u32x4 __attribute__((ext_vector_type(4)));
typedef unsigned u32x2 __attribute__((ext_vector_type(2)));
typedef unsigned short u16;

constexpr int S_ = 2048, DM = 576, DC = 512, NH = 32, HD = 128, NQ = 4096, DFF = 4608;

__device__ __forceinline__ unsigned cvtpk(float lo, float hi) {
    unsigned r; asm volatile("v_cvt_pk_bf16_f32 %0, %1, %2" : "=v"(r) : "v"(lo), "v"(hi)); return r;
}
__device__ __forceinline__ int crow(int r, int hi) { return (r & 3) + 8 * (r >> 2) + 4 * hi; }
__device__ __forceinline__ int v_rd_base(int lane) { return ((lane & 3) << 3) | (((lane >> 2) & 3) << 6) | (((lane >> 4) & 1) << 5) | (((lane >> 5) & 1) << 8); }
#define SBAR() __builtin_amdgcn_sched_barrier(0)

__device__ __forceinline__ void glds16(const void* g, void* l) {
    __builtin_amdgcn_global_load_lds((const __attribute__((address_space(1))) void*)g, (__attribute__((address_space(3))) void*)l, 16, 0, 0);
}

template <int BM, int BN, int WM, int WN, bool SWAP>
struct Gemm {
    static constexpr int BK = 64;
    static constexpr int TM = BM / WM / 32, TN = BN / WN / 32;
    static constexpr int A_BYTES = BM * BK * 2, B_BYTES = BN * BK * 2, STG = A_BYTES + B_BYTES;
    static constexpr int NA = BM * 8 / 512, NB = BN * 8 / 512, CB = BN / 32;
    __device__ static __forceinline__ void stage(const u16* A, int lda, const u16* B, int ldb, int k0, char* buf, int tid) {
        const int w = tid >> 6, lane = tid & 63;
#pragma unroll
        for (int i = 0; i < NA; ++i) { const int c = (i * 8 + w) * 64 + lane, row = c >> 3, sl = c & 7, sc = sl ^ ((row >> 1) & 7);
            glds16(A + (size_t)row * lda + k0 + sc * 8, buf + (i * 8 + w) * 1024); }
#pragma unroll
        for (int i = 0; i < NB; ++i) { const int c = (i * 8 + w) * 64 + lane, s = c >> 5, rk = (c >> 2) & 7, cc = c & 3;
            const int kh = s / CB, cb = s - kh * CB, kk = kh * 8 + rk, k = (kk & ~0xC) | ((kk & 4) << 1) | ((kk & 8) >> 1);
            glds16(B + (size_t)(k0 + k) * ldb + cb * 32 + cc * 8, buf + A_BYTES + (i * 8 + w) * 1024); }
    }
    __device__ static __forceinline__ void compute(const char* buf, int wm, int wn, int lane, f32x16 (&acc)[TM][TN]) {
        const int r32 = lane & 31, hi = lane >> 5;
        const int bb = (int)(uintptr_t)(buf + A_BYTES) + v_rd_base(lane) + wn * TN * 512;
#pragma unroll
        for (int ks = 0; ks < 4; ++ks) {
            bf16x8 af[TM]; s16x4 bl[TN], bh[TN];
#pragma unroll
            for (int t = 0; t < TN; ++t) {
                asm volatile("ds_read_b64_tr_b16 %0, %1 offset:%2" : "=&v"(bl[t]) : "v"(bb), "i"((ks * 2 * CB + t) * 512) : "memory");
                asm volatile("ds_read_b64_tr_b16 %0, %1 offset:%2" : "=&v"(bh[t]) : "v"(bb), "i"(((ks * 2 + 1) * CB + t) * 512) : "memory"); }
#pragma unroll
            for (int t = 0; t < TM; ++t) { const int row = (wm * TM + t) * 32 + r32;
                af[t] = *(const bf16x8*)(buf + row * 128 + (((ks * 2 + hi) ^ ((row >> 1) & 7)) << 4)); }
            asm volatile("s_waitcnt lgkmcnt(0)" ::: "memory"); SBAR();
#pragma unroll
            for (int tn = 0; tn < TN; ++tn) { const bf16x8 bf = {bl[tn][0], bl[tn][1], bl[tn][2], bl[tn][3], bh[tn][0], bh[tn][1], bh[tn][2], bh[tn][3]};
#pragma unroll
                for (int tm = 0; tm < TM; ++tm) {
                    if (SWAP) acc[tm][tn] = __builtin_amdgcn_mfma_f32_32x32x16_bf16(bf, af[tm], acc[tm][tn], 0, 0, 0);
                    else      acc[tm][tn] = __builtin_amdgcn_mfma_f32_32x32x16_bf16(af[tm], bf, acc[tm][tn], 0, 0, 0); } }
        }
    }
    __device__ static __forceinline__ void run(const u16* A, int lda, const u16* B, int ldb, int k0, int nst, char* lds, f32x16 (&acc)[TM][TN]) {
        const int tid = threadIdx.x, w = __builtin_amdgcn_readfirstlane(tid >> 6), lane = tid & 63, wm = w / WN, wn = w % WN;
#pragma unroll
        for (int i = 0; i < TM; ++i)
#pragma unroll
            for (int j = 0; j < TN; ++j) acc[i][j] = f32x16{};
        stage(A, lda, B, ldb, k0, lds, tid);
        asm volatile("s_waitcnt vmcnt(0)" ::: "memory"); __builtin_amdgcn_s_barrier();
        for (int t = 0; t < nst; ++t) {
            char* cur = lds + (t & 1) * STG; char* nxt = lds + ((t + 1) & 1) * STG;
            if (t + 1 < nst) stage(A, lda, B, ldb, k0 + (t + 1) * 64, nxt, tid);
            compute(cur, wm, wn, lane, acc);
            asm volatile("s_waitcnt vmcnt(0) lgkmcnt(0)" ::: "memory"); __builtin_amdgcn_s_barrier();
        }
    }
};

#define K1BN 256
constexpr int G1_BM = 128, G1_BN = K1BN, G1_WC = G1_BN / 4, G1_ST = G1_WC * 2 + 16;
constexpr int G1_LDS = 2 * (G1_BM + G1_BN) * 128;
__global__ __launch_bounds__(512, 1) void k1_proj(const u16* __restrict__ x, const u16* __restrict__ ckv, const u16* __restrict__ wq,
        const u16* __restrict__ wuk, const u16* __restrict__ wuv, const u16* __restrict__ w1, u16* __restrict__ qkv, u16* __restrict__ hbuf) {
    extern __shared__ __attribute__((aligned(16))) char lds[];
    using G = Gemm<G1_BM, G1_BN, 2, 4, true>;
    constexpr int NT = NQ / G1_BN;
    int b = blockIdx.x; const int mt = b & 15; int nt = b >> 4;
    const u16* A; const u16* B; u16* C; int lda, ldb, ldc, nst; bool relu = false;
    if (nt < NT) { A = x; lda = DM; B = wq; ldb = NQ; C = qkv; ldc = NQ; nst = 9; }
    else if (nt < 2 * NT) { nt -= NT; A = ckv; lda = DC; B = wuk; ldb = NQ; C = qkv + (size_t)S_ * NQ; ldc = NQ; nst = 8; }
    else if (nt < 3 * NT) { nt -= 2 * NT; A = ckv; lda = DC; B = wuv; ldb = NQ; C = qkv + (size_t)2 * S_ * NQ; ldc = NQ; nst = 8; }
    else { nt -= 3 * NT; A = x; lda = DM; B = w1; ldb = DFF; C = hbuf; ldc = DFF; nst = 9; relu = true; }
    f32x16 acc[G::TM][G::TN];
    G::run(A + (size_t)mt * G1_BM * lda, lda, B + nt * G1_BN, ldb, 0, nst, lds, acc);
    const int tid = threadIdx.x, w = tid >> 6, lane = tid & 63, r32 = lane & 31, hi = lane >> 5, wm = w >> 2, wn = w & 3;
    char* ep = lds + w * (64 * G1_ST);
#pragma unroll
    for (int tm = 0; tm < G::TM; ++tm)
#pragma unroll
        for (int tn = 0; tn < G::TN; ++tn)
#pragma unroll
            for (int q = 0; q < 4; ++q) { f32x16& a = acc[tm][tn]; float v0 = a[4 * q], v1 = a[4 * q + 1], v2 = a[4 * q + 2], v3 = a[4 * q + 3];
                if (relu) { v0 = fmaxf(v0, 0.f); v1 = fmaxf(v1, 0.f); v2 = fmaxf(v2, 0.f); v3 = fmaxf(v3, 0.f); }
                u32x2 pk = {cvtpk(v0, v1), cvtpk(v2, v3)};
                *(u32x2*)(ep + (tm * 32 + r32) * G1_ST + (tn * 32 + 8 * q + 4 * hi) * 2) = pk; }
    asm volatile("s_waitcnt lgkmcnt(0)" ::: "memory");
    u16* Cw = C + (size_t)(mt * G1_BM + wm * 64) * ldc + nt * G1_BN + wn * G1_WC;
    constexpr int CPR = G1_WC / 8, RPI = 64 / CPR;
#pragma unroll
    for (int i = 0; i < 64 / RPI; ++i) { const int row = i * RPI + lane / CPR, ch = lane % CPR;
        const u32x4 v = *(const u32x4*)(ep + row * G1_ST + ch * 16);
        *(u32x4*)(Cw + (size_t)row * ldc + ch * 8) = v; }
}

constexpr int G3_LDS = 2 * (256 * 128 + 192 * 128);
__global__ __launch_bounds__(512, 1) void k3_out(const u16* __restrict__ ctx, const u16* __restrict__ wo, const u16* __restrict__ hbuf,
        const u16* __restrict__ w2, float* __restrict__ part) {
    extern __shared__ __attribute__((aligned(16))) char lds[];
    using G = Gemm<256, 192, 4, 2, false>;
    const int b = blockIdx.x, tile = b % 24, sp = b / 24, mt = tile & 7, nt = tile >> 3;
    const u16* A; const u16* B; int lda, k0, nst;
    if (sp < 5) { A = ctx; lda = NQ; B = wo; const int s0 = (sp * 64) / 5, s1 = ((sp + 1) * 64) / 5; k0 = s0 * 64; nst = s1 - s0; }
    else { const int p = sp - 5; A = hbuf; lda = DFF; B = w2; const int s0 = (p * 72) / 5, s1 = ((p + 1) * 72) / 5; k0 = s0 * 64; nst = s1 - s0; }
    f32x16 acc[G::TM][G::TN];
    G::run(A + (size_t)mt * 256 * lda, lda, B + nt * 192, DM, k0, nst, lds, acc);
    const int tid = threadIdx.x, w = tid >> 6, lane = tid & 63, r32 = lane & 31, hi = lane >> 5, wm = w >> 1, wn = w & 1;
    float* P = part + (size_t)sp * S_ * DM + (size_t)(mt * 256 + wm * 64) * DM + nt * 192 + wn * 96 + r32;
#pragma unroll
    for (int tm = 0; tm < 2; ++tm)
#pragma unroll
        for (int tn = 0; tn < 3; ++tn)
#pragma unroll
            for (int r = 0; r < 16; ++r) P[(size_t)(tm * 32 + crow(r, hi)) * DM + tn * 32] = acc[tm][tn][r];
}

__device__ __forceinline__ float bfr(float v) { return __uint_as_float(cvtpk(v, v) << 16); }
__device__ __forceinline__ float bf2f(u16 v) { return __uint_as_float((unsigned)v << 16); }
__global__ __launch_bounds__(256) void k4_final(const float* __restrict__ part, const u16* __restrict__ x, const u16* __restrict__ gw, u16* __restrict__ out) {
    const int wv = threadIdx.x >> 6, lane = threadIdx.x & 63, row = blockIdx.x * 4 + wv;
    float s = 0.f;
    for (int i = lane; i < DM; i += 64) s += bfr(bf2f(x[(size_t)row * DM + i]) * bf2f(gw[i]));
    for (int o = 32; o > 0; o >>= 1) s += __shfl_xor(s, o);
    s = bfr(s);
    const float g = bfr(1.f / (1.f + __expf(-s))), g1 = bfr(1.f - g);
    for (int c = lane * 4; c < DM; c += 256) {
        f32x4 a = {0, 0, 0, 0}, bsum = {0, 0, 0, 0};
#pragma unroll
        for (int p = 0; p < 5; ++p) { a += *(const f32x4*)(part + ((size_t)p * S_ + row) * DM + c); bsum += *(const f32x4*)(part + ((size_t)(p + 5) * S_ + row) * DM + c); }
        float o4[4];
#pragma unroll
        for (int j = 0; j < 4; ++j) o4[j] = bfr(g * bfr(a[j])) + bfr(g1 * bfr(bsum[j]));
        u32x2 pk = {cvtpk(o4[0], o4[1]), cvtpk(o4[2], o4[3])};
        *(u32x2*)(out + (size_t)row * DM + c) = pk;
    }
}

constexpr float SCALE = 0.08838834764831845f;
constexpr int AT_BUF = 65536;
constexpr int AT_LDS = 2 * AT_BUF + 8 * 256;
__device__ __forceinline__ void attn_stage(const u16* Kh, const u16* Vh, int key0, char* buf, int tid) {
    const int w = tid >> 6, lane = tid & 63;
    int ko = (w * 4 + (lane >> 4)) * NQ + (((lane & 15) ^ ((lane >> 4) & 3) ^ ((w & 1) << 2)) << 3);
    const int kk = (w >> 1) * 8 + ((lane >> 2) & 7), k = (kk & ~0xC) | ((kk & 4) << 1) | ((kk & 8) >> 1);
    int vo = k * NQ + ((w & 1) * 2 + (lane >> 5)) * 32 + (lane & 3) * 8;
    asm volatile("" : "+v"(ko), "+v"(vo));
    const u16* kp = Kh + (size_t)key0 * NQ; const u16* vp = Vh + (size_t)key0 * NQ;
#pragma unroll
    for (int i = 0; i < 4; ++i) { glds16(kp + (size_t)i * 32 * NQ + ko, buf + (i * 8 + w) * 1024); glds16(vp + (size_t)i * 32 * NQ + vo, buf + 32768 + (i * 8 + w) * 1024); }
}
__global__ __launch_bounds__(512, 1) void k2_attn(const u16* __restrict__ qkv, u16* __restrict__ ctx) {
    extern __shared__ __attribute__((aligned(16))) char lds[];
    const int tid = threadIdx.x, w = __builtin_amdgcn_readfirstlane(tid >> 6), lane = tid & 63, r32 = lane & 31, hi = lane >> 5;
    const int grp = w >> 2, wq = w & 3;
    const int head = blockIdx.x >> 3, pr = blockIdx.x & 7;
    const u16* Qh = qkv + head * HD; const u16* Kh = qkv + (size_t)S_ * NQ + head * HD; const u16* Vh = qkv + (size_t)2 * S_ * NQ + head * HD;
    float* sm = (float*)(lds + 2 * AT_BUF) + w * 64;
    constexpr float C2 = 1.4426950408889634f * SCALE;
    for (int pass = 0; pass < 2; ++pass) {
        const int xb = pass == 0 ? 15 - pr : pr, q0 = xb * 128 + wq * 32, nit = xb + 1;
        bf16x8 qr[8];
#pragma unroll
        for (int d0 = 0; d0 < 8; ++d0) qr[d0] = *(const bf16x8*)(Qh + (size_t)(q0 + r32) * NQ + d0 * 16 + hi * 8);
        float m_reg = -1e30f, l_reg = 0.f; f32x16 o[4] = {};
        attn_stage(Kh, Vh, 0, lds, tid);
        asm volatile("s_waitcnt vmcnt(0)" ::: "memory"); __builtin_amdgcn_s_barrier();
        for (int it = 0; it < nit; ++it) {
            const char* cur = lds + (it & 1) * AT_BUF;
            if (it + 1 < nit) attn_stage(Kh, Vh, (it + 1) * 128, lds + ((it + 1) & 1) * AT_BUF, tid);
            const char* Kt = cur + grp * 16384; const int vb0 = (int)(uintptr_t)(cur + 32768 + grp * 16384) + v_rd_base(lane);
            f32x16 p0 = {}, p1 = {};
#pragma unroll
            for (int dd = 0; dd < 4; ++dd) { const char* a = Kt + r32 * 256 + (((dd * 2 + hi) ^ (r32 & 7)) << 4);
#pragma unroll
                for (int e = 0; e < 2; ++e) {
                const bf16x8 b0 = *(const bf16x8*)(a + e * 128), b1 = *(const bf16x8*)(a + e * 128 + 8192);
                p0 = __builtin_amdgcn_mfma_f32_32x32x16_bf16(b0, qr[dd + 4 * e], p0, 0, 0, 0);
                p1 = __builtin_amdgcn_mfma_f32_32x32x16_bf16(b1, qr[dd + 4 * e], p1, 0, 0, 0); } }
            const int kb = (2 * it + grp) * 64;
            if (kb + 63 > q0) { const int dq = q0 + r32 - 4 * hi - kb; const float NEG = -__builtin_inff();
#pragma unroll
                for (int r = 0; r < 16; ++r) { const int c = (r & 3) + 8 * (r >> 2); if (dq - c < 0) p0[r] = NEG; if (dq - c - 32 < 0) p1[r] = NEG; } }
            float pmax = p0[0];
#pragma unroll
            for (int r = 1; r < 16; ++r) pmax = fmaxf(pmax, p0[r]);
#pragma unroll
            for (int r = 0; r < 16; ++r) pmax = fmaxf(pmax, p1[r]);
            { auto rr = __builtin_amdgcn_permlane32_swap(__float_as_uint(pmax), __float_as_uint(pmax), false, false);
              pmax = fmaxf(__uint_as_float(rr[0]), __uint_as_float(rr[1])); }
            const float mn = fmaxf(m_reg, pmax), alpha = __builtin_amdgcn_exp2f((m_reg - mn) * C2); m_reg = mn;
            const float mnL = -mn * C2; float ps = 0.f;
#pragma unroll
            for (int r = 0; r < 16; ++r) { p0[r] = __builtin_amdgcn_exp2f(fmaf(p0[r], C2, mnL)); p1[r] = __builtin_amdgcn_exp2f(fmaf(p1[r], C2, mnL)); ps += p0[r] + p1[r]; }
            { auto rr = __builtin_amdgcn_permlane32_swap(__float_as_uint(ps), __float_as_uint(ps), false, false);
              ps = __uint_as_float(rr[0]) + __uint_as_float(rr[1]); }
            l_reg = l_reg * alpha + ps;
            if (__any(alpha < 1.f)) { if (hi == 0) sm[r32] = alpha; asm volatile("s_waitcnt lgkmcnt(0)" ::: "memory");
#pragma unroll
                for (int d_ = 0; d_ < 4; ++d_)
#pragma unroll
                    for (int r = 0; r < 16; ++r) o[d_][r] *= sm[crow(r, hi)]; }
            bf16x8 pa[4];
#define PK4(P, B_, OUT) do { unsigned a0 = cvtpk(P[B_+0], P[B_+1]), a1 = cvtpk(P[B_+2], P[B_+3]), b0 = cvtpk(P[B_+4], P[B_+5]), b1 = cvtpk(P[B_+6], P[B_+7]); \
        auto r0 = __builtin_amdgcn_permlane32_swap(a0, b0, false, false); auto r1 = __builtin_amdgcn_permlane32_swap(a1, b1, false, false); \
        u32x4 ww = {r0[0], r1[0], r0[1], r1[1]}; OUT = *reinterpret_cast<bf16x8*>(&ww); } while (0)
            PK4(p0, 0, pa[0]); PK4(p0, 8, pa[1]); PK4(p1, 0, pa[2]); PK4(p1, 8, pa[3]);
#undef PK4
#pragma unroll
            for (int dh = 0; dh < 8; ++dh) { const int d0 = dh >> 1, k2 = (dh & 1) * 2; s16x4 lo[4], hh[4];
#pragma unroll
                for (int ks = k2; ks < k2 + 2; ++ks) {
                    asm volatile("ds_read_b64_tr_b16 %0, %1 offset:%2" : "=&v"(lo[ks]) : "v"(vb0), "i"(d0 * 512 + ks * 4096) : "memory");
                    asm volatile("ds_read_b64_tr_b16 %0, %1 offset:%2" : "=&v"(hh[ks]) : "v"(vb0), "i"(d0 * 512 + ks * 4096 + 2048) : "memory"); }
                asm volatile("s_waitcnt lgkmcnt(0)" ::: "memory"); SBAR();
#pragma unroll
                for (int ks = k2; ks < k2 + 2; ++ks) o[d0] = __builtin_amdgcn_mfma_f32_32x32x16_bf16(pa[ks],
                    (bf16x8){lo[ks][0], lo[ks][1], lo[ks][2], lo[ks][3], hh[ks][0], hh[ks][1], hh[ks][2], hh[ks][3]}, o[d0], 0, 0, 0); }
            asm volatile("s_waitcnt vmcnt(0) lgkmcnt(0)" ::: "memory"); __builtin_amdgcn_s_barrier();
        }
        float* ob = (float*)lds + wq * 4096; float* ml = (float*)(lds + 2 * AT_BUF) + (wq + 4) * 64;
        if (grp == 1) { if (hi == 0) { ml[r32] = m_reg; ml[32 + r32] = l_reg; }
#pragma unroll
            for (int d0 = 0; d0 < 4; ++d0)
#pragma unroll
                for (int r = 0; r < 16; ++r) ob[crow(r, hi) * 128 + d0 * 32 + r32] = o[d0][r]; }
        asm volatile("s_waitcnt lgkmcnt(0)" ::: "memory"); __builtin_amdgcn_s_barrier();
        if (grp == 0) {
            const float m1 = ml[r32], l1 = ml[32 + r32], mn = fmaxf(m_reg, m1);
            const float a0 = __builtin_amdgcn_exp2f((m_reg - mn) * C2), a1 = __builtin_amdgcn_exp2f((m1 - mn) * C2), rl = 1.f / (l_reg * a0 + l1 * a1);
            if (hi == 0) { sm[r32] = a0 * rl; sm[32 + r32] = a1 * rl; } asm volatile("s_waitcnt lgkmcnt(0)" ::: "memory");
            u16* Ow = ctx + (size_t)q0 * NQ + head * HD;
#pragma unroll
            for (int r = 0; r < 16; ++r) { const int orow = crow(r, hi); const float f0 = sm[orow], f1 = sm[32 + orow];
#pragma unroll
                for (int d0 = 0; d0 < 4; ++d0) { const float v = o[d0][r] * f0 + ob[orow * 128 + d0 * 32 + r32] * f1; const float vn = __shfl_xor(v, 1);
                    if ((r32 & 1) == 0) *(unsigned*)(Ow + (size_t)orow * NQ + d0 * 32 + r32) = cvtpk(v, vn); } }
        }
        __syncthreads();
    }
}

extern "C" void kernel_launch(void* const* d_in, const int* in_sizes, int n_in, void* d_out, int out_size, void* d_ws, size_t ws_size, hipStream_t stream) {
    const u16* x = (const u16*)d_in[0]; const u16* ckv = (const u16*)d_in[1]; const u16* wq = (const u16*)d_in[2]; const u16* wuk = (const u16*)d_in[3];
    const u16* wuv = (const u16*)d_in[4]; const u16* wo = (const u16*)d_in[5]; const u16* w1 = (const u16*)d_in[7]; const u16* w2 = (const u16*)d_in[8];
    const u16* gw = (const u16*)d_in[9];
    char* ws = (char*)d_ws;
    u16* qkv = (u16*)ws;
    u16* hbuf = (u16*)(ws + (size_t)48 * 1048576);
    u16* ctx = (u16*)(ws + (size_t)68 * 1048576);
    float* part = (float*)(ws + (size_t)84 * 1048576);
    hipFuncSetAttribute((const void*)k1_proj, hipFuncAttributeMaxDynamicSharedMemorySize, G1_LDS);
    hipFuncSetAttribute((const void*)k2_attn, hipFuncAttributeMaxDynamicSharedMemorySize, AT_LDS);
    hipFuncSetAttribute((const void*)k3_out, hipFuncAttributeMaxDynamicSharedMemorySize, G3_LDS);
    k1_proj<<<16 * (3 * NQ + DFF) / G1_BN, 512, G1_LDS, stream>>>(x, ckv, wq, wuk, wuv, w1, qkv, hbuf);
    k2_attn<<<256, 512, AT_LDS, stream>>>(qkv, ctx);
    k3_out<<<240, 512, G3_LDS, stream>>>(ctx, wo, hbuf, w2, part);
    k4_final<<<512, 256, 0, stream>>>(part, x, gw, (u16*)d_out);
}
```

```cpp
#include <hip/hip_runtime.h>
#include <hip/hip_bf16.h>
#include <cstdint>
#include <type_traits>

typedef short bf16x8 __attribute__((ext_vector_type(8)));
typedef short s16x4 __attribute__((ext_vector_type(4)));
typedef float f32x16 __attribute__((ext_vector_type(16)));
typedef float f32x4 __attribute__((ext_vector_type(4)));
typedef unsigned u32x4 __attribute__((ext_vector_type(4)));
typedef unsigned u32x2 __attribute__((ext_vector_type(2)));
typedef unsigned short u16;

constexpr int S_ = 2048, DM = 576, DC = 512, NH = 32, HD = 128, NQ = 4096, DFF = 4608;

__device__ __forceinline__ unsigned cvtpk(float lo, float hi) {
    unsigned r; asm volatile("v_cvt_pk_bf16_f32 %0, %1, %2" : "=v"(r) : "v"(lo), "v"(hi)); return r;
}
__device__ __forceinline__ int crow(int r, int hi) { return (r & 3) + 8 * (r >> 2) + 4 * hi; }
__device__ __forceinline__ int v_rd_base(int lane) { return ((lane & 3) << 3) | (((lane >> 2) & 3) << 6) | (((lane >> 4) & 1) << 5) | (((lane >> 5) & 1) << 8); }
#define SBAR() __builtin_amdgcn_sched_barrier(0)

__device__ __forceinline__ void glds16(const void* g, void* l) {
    __builtin_amdgcn_global_load_lds((const __attribute__((address_space(1))) void*)g, (__attribute__((address_space(3))) void*)l, 16, 0, 0);
}

template <int BM, int BN, int WM, int WN, bool SWAP>
struct Gemm {
    static constexpr int BK = 64;
    static constexpr int TM = BM / WM / 32, TN = BN / WN / 32;
    static constexpr int A_BYTES = BM * BK * 2, B_BYTES = BN * BK * 2, STG = A_BYTES + B_BYTES;
    static constexpr int NA = BM * 8 / 512, NB = BN * 8 / 512, CB = BN / 32;
    __device__ static __forceinline__ void stage(const u16* A, int lda, const u16* B, int ldb, int k0, char* buf, int tid) {
        const int w = tid >> 6, lane = tid & 63;
#pragma unroll
        for (int i = 0; i < NA; ++i) { const int c = (i * 8 + w) * 64 + lane, row = c >> 3, sl = c & 7, sc = sl ^ ((row >> 1) & 7);
            glds16(A + (size_t)row * lda + k0 + sc * 8, buf + (i * 8 + w) * 1024); }
#pragma unroll
        for (int i = 0; i < NB; ++i) { const int c = (i * 8 + w) * 64 + lane, s = c >> 5, rk = (c >> 2) & 7, cc = c & 3;
            const int kh = s / CB, cb = s - kh * CB, kk = kh * 8 + rk, k = (kk & ~0xC) | ((kk & 4) << 1) | ((kk & 8) >> 1);
            glds16(B + (size_t)(k0 + k) * ldb + cb * 32 + cc * 8, buf + A_BYTES + (i * 8 + w) * 1024); }
    }
    __device__ static __forceinline__ void compute(const char* buf, int wm, int wn, int lane, f32x16 (&acc)[TM][TN]) {
        const int r32 = lane & 31, hi = lane >> 5;
        const int bb = (int)(uintptr_t)(buf + A_BYTES) + v_rd_base(lane) + wn * TN * 512;
#pragma unroll
        for (int ks = 0; ks < 4; ++ks) {
            bf16x8 af[TM]; s16x4 bl[TN], bh[TN];
#pragma unroll
            for (int t = 0; t < TN; ++t) {
                asm volatile("ds_read_b64_tr_b16 %0, %1 offset:%2" : "=&v"(bl[t]) : "v"(bb), "i"((ks * 2 * CB + t) * 512) : "memory");
                asm volatile("ds_read_b64_tr_b16 %0, %1 offset:%2" : "=&v"(bh[t]) : "v"(bb), "i"(((ks * 2 + 1) * CB + t) * 512) : "memory"); }
#pragma unroll
            for (int t = 0; t < TM; ++t) { const int row = (wm * TM + t) * 32 + r32;
                af[t] = *(const bf16x8*)(buf + row * 128 + (((ks * 2 + hi) ^ ((row >> 1) & 7)) << 4)); }
            asm volatile("s_waitcnt lgkmcnt(0)" ::: "memory"); SBAR();
#pragma unroll
            for (int tn = 0; tn < TN; ++tn) { const bf16x8 bf = {bl[tn][0], bl[tn][1], bl[tn][2], bl[tn][3], bh[tn][0], bh[tn][1], bh[tn][2], bh[tn][3]};
#pragma unroll
                for (int tm = 0; tm < TM; ++tm) {
                    if (SWAP) acc[tm][tn] = __builtin_amdgcn_mfma_f32_32x32x16_bf16(bf, af[tm], acc[tm][tn], 0, 0, 0);
                    else      acc[tm][tn] = __builtin_amdgcn_mfma_f32_32x32x16_bf16(af[tm], bf, acc[tm][tn], 0, 0, 0); } }
        }
    }
    __device__ static __forceinline__ void run(const u16* A, int lda, const u16* B, int ldb, int k0, int nst, char* lds, f32x16 (&acc)[TM][TN]) {
        const int tid = threadIdx.x, w = __builtin_amdgcn_readfirstlane(tid >> 6), lane = tid & 63, wm = w / WN, wn = w % WN;
#pragma unroll
        for (int i = 0; i < TM; ++i)
#pragma unroll
            for (int j = 0; j < TN; ++j) acc[i][j] = f32x16{};
        stage(A, lda, B, ldb, k0, lds, tid);
        asm volatile("s_waitcnt vmcnt(0)" ::: "memory"); __builtin_amdgcn_s_barrier();
        for (int t = 0; t < nst; ++t) {
            char* cur = lds + (t & 1) * STG; char* nxt = lds + ((t + 1) & 1) * STG;
            if (t + 1 < nst) stage(A, lda, B, ldb, k0 + (t + 1) * 64, nxt, tid);
            compute(cur, wm, wn, lane, acc);
            asm volatile("s_waitcnt vmcnt(0) lgkmcnt(0)" ::: "memory"); __builtin_amdgcn_s_barrier();
        }
    }
};

#define K1BN 128
constexpr int G1_BM = 128, G1_BN = K1BN, G1_WC = G1_BN / 4, G1_ST = G1_WC * 2 + 16;
constexpr int G1_LDS = 2 * (G1_BM + G1_BN) * 128;
__global__ __launch_bounds__(512, 1) void k1_proj(const u16* __restrict__ x, const u16* __restrict__ ckv, const u16* __restrict__ wq,
        const u16* __restrict__ wuk, const u16* __restrict__ wuv, const u16* __restrict__ w1, u16* __restrict__ qkv, u16* __restrict__ hbuf) {
    extern __shared__ __attribute__((aligned(16))) char lds[];
    using G = Gemm<G1_BM, G1_BN, 2, 4, true>;
    constexpr int NT = NQ / G1_BN;
    int b = blockIdx.x; const int mt = b & 15; int nt = b >> 4;
    const u16* A; const u16* B; u16* C; int lda, ldb, ldc, nst; bool relu = false;
    if (nt < NT) { A = x; lda = DM; B = wq; ldb = NQ; C = qkv; ldc = NQ; nst = 9; }
    else if (nt < 2 * NT) { nt -= NT; A = ckv; lda = DC; B = wuk; ldb = NQ; C = qkv + (size_t)S_ * NQ; ldc = NQ; nst = 8; }
    else if (nt < 3 * NT) { nt -= 2 * NT; A = ckv; lda = DC; B = wuv; ldb = NQ; C = qkv + (size_t)2 * S_ * NQ; ldc = NQ; nst = 8; }
    else { nt -= 3 * NT; A = x; lda = DM; B = w1; ldb = DFF; C = hbuf; ldc = DFF; nst = 9; relu = true; }
    f32x16 acc[G::TM][G::TN];
    G::run(A + (size_t)mt * G1_BM * lda, lda, B + nt * G1_BN, ldb, 0, nst, lds, acc);
    const int tid = threadIdx.x, w = tid >> 6, lane = tid & 63, r32 = lane & 31, hi = lane >> 5, wm = w >> 2, wn = w & 3;
    char* ep = lds + w * (64 * G1_ST);
#pragma unroll
    for (int tm = 0; tm < G::TM; ++tm)
#pragma unroll
        for (int tn = 0; tn < G::TN; ++tn)
#pragma unroll
            for (int q = 0; q < 4; ++q) { f32x16& a = acc[tm][tn]; float v0 = a[4 * q], v1 = a[4 * q + 1], v2 = a[4 * q + 2], v3 = a[4 * q + 3];
                if (relu) { v0 = fmaxf(v0, 0.f); v1 = fmaxf(v1, 0.f); v2 = fmaxf(v2, 0.f); v3 = fmaxf(v3, 0.f); }
                u32x2 pk = {cvtpk(v0, v1), cvtpk(v2, v3)};
                *(u32x2*)(ep + (tm * 32 + r32) * G1_ST + (tn * 32 + 8 * q + 4 * hi) * 2) = pk; }
    asm volatile("s_waitcnt lgkmcnt(0)" ::: "memory");
    u16* Cw = C + (size_t)(mt * G1_BM + wm * 64) * ldc + nt * G1_BN + wn * G1_WC;
    constexpr int CPR = G1_WC / 8, RPI = 64 / CPR;
#pragma unroll
    for (int i = 0; i < 64 / RPI; ++i) { const int row = i * RPI + lane / CPR, ch = lane % CPR;
        const u32x4 v = *(const u32x4*)(ep + row * G1_ST + ch * 16);
        *(u32x4*)(Cw + (size_t)row * ldc + ch * 8) = v; }
}

constexpr int G3_LDS = 2 * (256 * 128 + 192 * 128);
__global__ __launch_bounds__(512, 1) void k3_out(const u16* __restrict__ ctx, const u16* __restrict__ wo, const u16* __restrict__ hbuf,
        const u16* __restrict__ w2, float* __restrict__ part) {
    extern __shared__ __attribute__((aligned(16))) char lds[];
    using G = Gemm<256, 192, 4, 2, false>;
    const int b = blockIdx.x, tile = b % 24, sp = b / 24, mt = tile & 7, nt = tile >> 3;
    const u16* A; const u16* B; int lda, k0, nst;
    if (sp < 5) { A = ctx; lda = NQ; B = wo; const int s0 = (sp * 64) / 5, s1 = ((sp + 1) * 64) / 5; k0 = s0 * 64; nst = s1 - s0; }
    else { const int p = sp - 5; A = hbuf; lda = DFF; B = w2; const int s0 = (p * 72) / 5, s1 = ((p + 1) * 72) / 5; k0 = s0 * 64; nst = s1 - s0; }
    f32x16 acc[G::TM][G::TN];
    G::run(A + (size_t)mt * 256 * lda, lda, B + nt * 192, DM, k0, nst, lds, acc);
    const int tid = threadIdx.x, w = tid >> 6, lane = tid & 63, r32 = lane & 31, hi = lane >> 5, wm = w >> 1, wn = w & 1;
    float* P = part + (size_t)sp * S_ * DM + (size_t)(mt * 256 + wm * 64) * DM + nt * 192 + wn * 96 + r32;
#pragma unroll
    for (int tm = 0; tm < 2; ++tm)
#pragma unroll
        for (int tn = 0; tn < 3; ++tn)
#pragma unroll
            for (int r = 0; r < 16; ++r) P[(size_t)(tm * 32 + crow(r, hi)) * DM + tn * 32] = acc[tm][tn][r];
}

__device__ __forceinline__ float bfr(float v) { return __uint_as_float(cvtpk(v, v) << 16); }
__device__ __forceinline__ float bf2f(u16 v) { return __uint_as_float((unsigned)v << 16); }
__global__ __launch_bounds__(256) void k4_final(const float* __restrict__ part, const u16* __restrict__ x, const u16* __restrict__ gw, u16* __restrict__ out) {
    const int wv = threadIdx.x >> 6, lane = threadIdx.x & 63, row = blockIdx.x * 4 + wv;
    float s = 0.f;
    for (int i = lane; i < DM; i += 64) s += bfr(bf2f(x[(size_t)row * DM + i]) * bf2f(gw[i]));
    for (int o = 32; o > 0; o >>= 1) s += __shfl_xor(s, o);
    s = bfr(s);
    const float g = bfr(1.f / (1.f + __expf(-s))), g1 = bfr(1.f - g);
    for (int c = lane * 4; c < DM; c += 256) {
        f32x4 a = {0, 0, 0, 0}, bsum = {0, 0, 0, 0};
#pragma unroll
        for (int p = 0; p < 5; ++p) { a += *(const f32x4*)(part + ((size_t)p * S_ + row) * DM + c); bsum += *(const f32x4*)(part + ((size_t)(p + 5) * S_ + row) * DM + c); }
        float o4[4];
#pragma unroll
        for (int j = 0; j < 4; ++j) o4[j] = bfr(g * bfr(a[j])) + bfr(g1 * bfr(bsum[j]));
        u32x2 pk = {cvtpk(o4[0], o4[1]), cvtpk(o4[2], o4[3])};
        *(u32x2*)(out + (size_t)row * DM + c) = pk;
    }
}

constexpr float SCALE = 0.08838834764831845f;
constexpr int AT_BUF = 65536;
constexpr int AT_LDS = 2 * AT_BUF + 8 * 256;
__device__ __forceinline__ void stage_k(const u16* Kh, int key0, char* buf, int tid) {
    const int w = tid >> 6, lane = tid & 63;
    int ko = (w * 4 + (lane >> 4)) * NQ + (((lane & 15) ^ ((lane >> 4) & 3) ^ ((w & 1) << 2)) << 3);
    asm volatile("" : "+v"(ko));
    const u16* kp = Kh + (size_t)key0 * NQ;
#pragma unroll
    for (int i = 0; i < 4; ++i) glds16(kp + (size_t)i * 32 * NQ + ko, buf + (i * 8 + w) * 1024);
}
__device__ __forceinline__ void stage_v(const u16* Vh, int key0, char* buf, int tid) {
    const int w = tid >> 6, lane = tid & 63;
    const int kk = (w >> 1) * 8 + ((lane >> 2) & 7), k = (kk & ~0xC) | ((kk & 4) << 1) | ((kk & 8) >> 1);
    int vo = k * NQ + ((w & 1) * 2 + (lane >> 5)) * 32 + (lane & 3) * 8;
    asm volatile("" : "+v"(vo));
    const u16* vp = Vh + (size_t)key0 * NQ;
#pragma unroll
    for (int i = 0; i < 4; ++i) glds16(vp + (size_t)i * 32 * NQ + vo, buf + 32768 + (i * 8 + w) * 1024);
}
__global__ __launch_bounds__(512, 1) void k2_attn(const u16* __restrict__ qkv, u16* __restrict__ ctx) {
    extern __shared__ __attribute__((aligned(16))) char lds[];
    const int tid = threadIdx.x, w = __builtin_amdgcn_readfirstlane(tid >> 6), lane = tid & 63, r32 = lane & 31, hi = lane >> 5;
    const int grp = w >> 2, wq = w & 3;
    const int head = (blockIdx.x & 7) * 4 + (blockIdx.x >> 6), pr = (blockIdx.x >> 3) & 7;
    const u16* Qh = qkv + head * HD; const u16* Kh = qkv + (size_t)S_ * NQ + head * HD; const u16* Vh = qkv + (size_t)2 * S_ * NQ + head * HD;
    float* sm = (float*)(lds + 2 * AT_BUF) + w * 64;
    constexpr float C2 = 1.4426950408889634f * SCALE;
    for (int pass = 0; pass < 2; ++pass) {
        const int xb = pass == 0 ? 15 - pr : pr, q0 = xb * 128 + wq * 32, nit = xb + 1;
        bf16x8 qr[8];
#pragma unroll
        for (int d0 = 0; d0 < 8; ++d0) qr[d0] = *(const bf16x8*)(Qh + (size_t)(q0 + r32) * NQ + d0 * 16 + hi * 8);
        float m_reg = -1e30f, l_reg = 0.f; f32x16 o[4] = {};
        stage_k(Kh, 0, lds, tid);
        asm volatile("s_waitcnt vmcnt(0)" ::: "memory"); __builtin_amdgcn_s_barrier();
        f32x16 y0, y1; float alpha = 1.f;
        auto step = [&](int it, auto DOQ, auto DOP) __attribute__((always_inline)) {
            const char* cur = lds + (it & 1) * AT_BUF;
            f32x16 p0 = {}, p1 = {};
            if constexpr (decltype(DOQ)::value) {
                if (it + 1 < nit) stage_k(Kh, (it + 1) * 128, lds + ((it + 1) & 1) * AT_BUF, tid);
                stage_v(Vh, it * 128, lds + (it & 1) * AT_BUF, tid);
                const char* Kt = cur + grp * 16384;
#pragma unroll
                for (int dd = 0; dd < 4; ++dd) { const char* a = Kt + r32 * 256 + (((dd * 2 + hi) ^ (r32 & 7)) << 4);
#pragma unroll
                    for (int e = 0; e < 2; ++e) {
                    const bf16x8 b0 = *(const bf16x8*)(a + e * 128), b1 = *(const bf16x8*)(a + e * 128 + 8192);
                    p0 = __builtin_amdgcn_mfma_f32_32x32x16_bf16(b0, qr[dd + 4 * e], p0, 0, 0, 0);
                    p1 = __builtin_amdgcn_mfma_f32_32x32x16_bf16(b1, qr[dd + 4 * e], p1, 0, 0, 0); } __builtin_amdgcn_sched_group_barrier(0x100, 4, 0); }
            }
            if constexpr (decltype(DOP)::value) {
                float ps = 0.f;
#pragma unroll
                for (int r = 0; r < 16; ++r) { y0[r] = __builtin_amdgcn_exp2f(y0[r]); y1[r] = __builtin_amdgcn_exp2f(y1[r]); ps += y0[r] + y1[r]; }
                { auto rr = __builtin_amdgcn_permlane32_swap(__float_as_uint(ps), __float_as_uint(ps), false, false);
                  ps = __uint_as_float(rr[0]) + __uint_as_float(rr[1]); }
                l_reg = l_reg * alpha + ps;
                bf16x8 pa[4];
#define PK4(P, B_, OUT) do { unsigned a0 = cvtpk(P[B_+0], P[B_+1]), a1 = cvtpk(P[B_+2], P[B_+3]), b0 = cvtpk(P[B_+4], P[B_+5]), b1 = cvtpk(P[B_+6], P[B_+7]); \
        auto r0 = __builtin_amdgcn_permlane32_swap(a0, b0, false, false); auto r1 = __builtin_amdgcn_permlane32_swap(a1, b1, false, false); \
        u32x4 ww = {r0[0], r1[0], r0[1], r1[1]}; OUT = *reinterpret_cast<bf16x8*>(&ww); } while (0)
                PK4(y0, 0, pa[0]); PK4(y0, 8, pa[1]); PK4(y1, 0, pa[2]); PK4(y1, 8, pa[3]);
#undef PK4
                const int vb0 = (int)(uintptr_t)(lds + ((it - 1) & 1) * AT_BUF + 32768 + grp * 16384) + v_rd_base(lane);
#pragma unroll
                for (int dh = 0; dh < 8; ++dh) { const int d0 = dh >> 1, k2 = (dh & 1) * 2; s16x4 lo[4], hh[4];
#pragma unroll
                    for (int ks = k2; ks < k2 + 2; ++ks) {
                        asm volatile("ds_read_b64_tr_b16 %0, %1 offset:%2" : "=&v"(lo[ks]) : "v"(vb0), "i"(d0 * 512 + ks * 4096) : "memory");
                        asm volatile("ds_read_b64_tr_b16 %0, %1 offset:%2" : "=&v"(hh[ks]) : "v"(vb0), "i"(d0 * 512 + ks * 4096 + 2048) : "memory"); }
                    asm volatile("s_waitcnt lgkmcnt(0)" ::: "memory"); SBAR();
#pragma unroll
                    for (int ks = k2; ks < k2 + 2; ++ks) o[d0] = __builtin_amdgcn_mfma_f32_32x32x16_bf16(pa[ks],
                        (bf16x8){lo[ks][0], lo[ks][1], lo[ks][2], lo[ks][3], hh[ks][0], hh[ks][1], hh[ks][2], hh[ks][3]}, o[d0], 0, 0, 0); }
            }
            if constexpr (decltype(DOQ)::value) {
                const int kb = (2 * it + grp) * 64;
                if (kb + 63 > q0) { const int dq = q0 + r32 - 4 * hi - kb; const float NEG = -__builtin_inff();
#pragma unroll
                    for (int r = 0; r < 16; ++r) { const int c = (r & 3) + 8 * (r >> 2); if (dq - c < 0) p0[r] = NEG; if (dq - c - 32 < 0) p1[r] = NEG; } }
                float pmax = p0[0];
#pragma unroll
                for (int r = 1; r < 16; ++r) pmax = fmaxf(pmax, p0[r]);
#pragma unroll
                for (int r = 0; r < 16; ++r) pmax = fmaxf(pmax, p1[r]);
                { auto rr = __builtin_amdgcn_permlane32_swap(__float_as_uint(pmax), __float_as_uint(pmax), false, false);
                  pmax = fmaxf(__uint_as_float(rr[0]), __uint_as_float(rr[1])); }
                const float mn = fmaxf(m_reg, pmax); alpha = __builtin_amdgcn_exp2f((m_reg - mn) * C2); m_reg = mn;
                const float mnL = -mn * C2;
#pragma unroll
                for (int r = 0; r < 16; ++r) { y0[r] = fmaf(p0[r], C2, mnL); y1[r] = fmaf(p1[r], C2, mnL); }
                if (__any(alpha < 1.f)) { if (hi == 0) sm[r32] = alpha; asm volatile("s_waitcnt lgkmcnt(0)" ::: "memory");
#pragma unroll
                    for (int d_ = 0; d_ < 4; ++d_)
#pragma unroll
                        for (int r = 0; r < 16; ++r) o[d_][r] *= sm[crow(r, hi)]; }
            }
            asm volatile("s_waitcnt vmcnt(0) lgkmcnt(0)" ::: "memory"); __builtin_amdgcn_s_barrier();
        };
        step(0, std::true_type{}, std::false_type{});
        for (int it = 1; it < nit; ++it) step(it, std::true_type{}, std::true_type{});
        step(nit, std::false_type{}, std::true_type{});
        float* ob = (float*)lds + wq * 4096; float* ml = (float*)(lds + 2 * AT_BUF) + (wq + 4) * 64;
        if (grp == 1) { if (hi == 0) { ml[r32] = m_reg; ml[32 + r32] = l_reg; }
#pragma unroll
            for (int d0 = 0; d0 < 4; ++d0)
#pragma unroll
                for (int r = 0; r < 16; ++r) ob[crow(r, hi) * 128 + d0 * 32 + r32] = o[d0][r]; }
        asm volatile("s_waitcnt lgkmcnt(0)" ::: "memory"); __builtin_amdgcn_s_barrier();
        if (grp == 0) {
            const float m1 = ml[r32], l1 = ml[32 + r32], mn = fmaxf(m_reg, m1);
            const float a0 = __builtin_amdgcn_exp2f((m_reg - mn) * C2), a1 = __builtin_amdgcn_exp2f((m1 - mn) * C2), rl = 1.f / (l_reg * a0 + l1 * a1);
            if (hi == 0) { sm[r32] = a0 * rl; sm[32 + r32] = a1 * rl; } asm volatile("s_waitcnt lgkmcnt(0)" ::: "memory");
            u16* Ow = ctx + (size_t)q0 * NQ + head * HD;
#pragma unroll
            for (int r = 0; r < 16; ++r) { const int orow = crow(r, hi); const float f0 = sm[orow], f1 = sm[32 + orow];
#pragma unroll
                for (int d0 = 0; d0 < 4; ++d0) { const float v = o[d0][r] * f0 + ob[orow * 128 + d0 * 32 + r32] * f1; const float vn = __shfl_xor(v, 1);
                    if ((r32 & 1) == 0) *(unsigned*)(Ow + (size_t)orow * NQ + d0 * 32 + r32) = cvtpk(v, vn); } }
        }
        __syncthreads();
    }
}

extern "C" void kernel_launch(void* const* d_in, const int* in_sizes, int n_in, void* d_out, int out_size, void* d_ws, size_t ws_size, hipStream_t stream) {
    const u16* x = (const u16*)d_in[0]; const u16* ckv = (const u16*)d_in[1]; const u16* wq = (const u16*)d_in[2]; const u16* wuk = (const u16*)d_in[3];
    const u16* wuv = (const u16*)d_in[4]; const u16* wo = (const u16*)d_in[5]; const u16* w1 = (const u16*)d_in[7]; const u16* w2 = (const u16*)d_in[8];
    const u16* gw = (const u16*)d_in[9];
    char* ws = (char*)d_ws;
    u16* qkv = (u16*)ws;
    u16* hbuf = (u16*)(ws + (size_t)48 * 1048576);
    u16* ctx = (u16*)(ws + (size_t)68 * 1048576);
    float* part = (float*)(ws + (size_t)84 * 1048576);
    hipFuncSetAttribute((const void*)k1_proj, hipFuncAttributeMaxDynamicSharedMemorySize, G1_LDS);
    hipFuncSetAttribute((const void*)k2_attn, hipFuncAttributeMaxDynamicSharedMemorySize, AT_LDS);
    hipFuncSetAttribute((const void*)k3_out, hipFuncAttributeMaxDynamicSharedMemorySize, G3_LDS);
    k1_proj<<<16 * (3 * NQ + DFF) / G1_BN, 512, G1_LDS, stream>>>(x, ckv, wq, wuk, wuv, w1, qkv, hbuf);
    k2_attn<<<256, 512, AT_LDS, stream>>>(qkv, ctx);
    k3_out<<<240, 512, G3_LDS, stream>>>(ctx, wo, hbuf, w2, part);
    k4_final<<<512, 256, 0, stream>>>(part, x, gw, (u16*)d_out);
}
```

```cpp
#include <hip/hip_runtime.h>
#include <hip/hip_bf16.h>
#include <cstdint>
#include <type_traits>

typedef short bf16x8 __attribute__((ext_vector_type(8)));
typedef short s16x4 __attribute__((ext_vector_type(4)));
typedef float f32x16 __attribute__((ext_vector_type(16)));
typedef float f32x4 __attribute__((ext_vector_type(4)));
typedef unsigned u32x4 __attribute__((ext_vector_type(4)));
typedef unsigned u32x2 __attribute__((ext_vector_type(2)));
typedef unsigned short u16;

constexpr int S_ = 2048, DM = 576, DC = 512, NH = 32, HD = 128, NQ = 4096, DFF = 4608;

__device__ __forceinline__ unsigned cvtpk(float lo, float hi) {
    unsigned r; asm volatile("v_cvt_pk_bf16_f32 %0, %1, %2" : "=v"(r) : "v"(lo), "v"(hi)); return r;
}
__device__ __forceinline__ int crow(int r, int hi) { return (r & 3) + 8 * (r >> 2) + 4 * hi; }
__device__ __forceinline__ int v_rd_base(int lane) { return ((lane & 3) << 3) | (((lane >> 2) & 3) << 6) | (((lane >> 4) & 1) << 5) | (((lane >> 5) & 1) << 8); }
#define SBAR() __builtin_amdgcn_sched_barrier(0)

__device__ __forceinline__ void glds16(const void* g, void* l) {
    __builtin_amdgcn_global_load_lds((const __attribute__((address_space(1))) void*)g, (__attribute__((address_space(3))) void*)l, 16, 0, 0);
}

template <int BM, int BN, int WM, int WN, bool SWAP, int NSTG>
struct Gemm {
    static constexpr int BK = 64;
    static constexpr int TM = BM / WM / 32, TN = BN / WN / 32;
    static constexpr int A_BYTES = BM * BK * 2, B_BYTES = BN * BK * 2, STG = A_BYTES + B_BYTES;
    static constexpr int NA = BM * 8 / 512, NB = BN * 8 / 512, CB = BN / 32;
    __device__ static __forceinline__ void stage(const u16* A, int lda, const u16* B, int ldb, int k0, char* buf, int tid) {
        const int w = tid >> 6, lane = tid & 63;
#pragma unroll
        for (int i = 0; i < NA; ++i) { const int c = (i * 8 + w) * 64 + lane, row = c >> 3, sl = c & 7, sc = sl ^ ((row >> 1) & 7);
            glds16(A + (size_t)row * lda + k0 + sc * 8, buf + (i * 8 + w) * 1024); }
#pragma unroll
        for (int i = 0; i < NB; ++i) { const int c = (i * 8 + w) * 64 + lane, s = c >> 5, rk = (c >> 2) & 7, cc = c & 3;
            const int kh = s / CB, cb = s - kh * CB, kk = kh * 8 + rk, k = (kk & ~0xC) | ((kk & 4) << 1) | ((kk & 8) >> 1);
            glds16(B + (size_t)(k0 + k) * ldb + cb * 32 + cc * 8, buf + A_BYTES + (i * 8 + w) * 1024); }
    }
    __device__ static __forceinline__ void compute(const char* buf, int wm, int wn, int lane, f32x16 (&acc)[TM][TN]) {
        const int r32 = lane & 31, hi = lane >> 5;
        const int bb = (int)(uintptr_t)(buf + A_BYTES) + v_rd_base(lane) + wn * TN * 512;
#pragma unroll
        for (int ks = 0; ks < 4; ++ks) {
            bf16x8 af[TM]; s16x4 bl[TN], bh[TN];
#pragma unroll
            for (int t = 0; t < TN; ++t) {
                asm volatile("ds_read_b64_tr_b16 %0, %1 offset:%2" : "=&v"(bl[t]) : "v"(bb), "i"((ks * 2 * CB + t) * 512) : "memory");
                asm volatile("ds_read_b64_tr_b16 %0, %1 offset:%2" : "=&v"(bh[t]) : "v"(bb), "i"(((ks * 2 + 1) * CB + t) * 512) : "memory"); }
#pragma unroll
            for (int t = 0; t < TM; ++t) { const int row = (wm * TM + t) * 32 + r32;
                af[t] = *(const bf16x8*)(buf + row * 128 + (((ks * 2 + hi) ^ ((row >> 1) & 7)) << 4)); }
            asm volatile("s_waitcnt lgkmcnt(0)" ::: "memory"); SBAR();
#pragma unroll
            for (int tn = 0; tn < TN; ++tn) { const bf16x8 bf = {bl[tn][0], bl[tn][1], bl[tn][2], bl[tn][3], bh[tn][0], bh[tn][1], bh[tn][2], bh[tn][3]};
#pragma unroll
                for (int tm = 0; tm < TM; ++tm) {
                    if (SWAP) acc[tm][tn] = __builtin_amdgcn_mfma_f32_32x32x16_bf16(bf, af[tm], acc[tm][tn], 0, 0, 0);
                    else      acc[tm][tn] = __builtin_amdgcn_mfma_f32_32x32x16_bf16(af[tm], bf, acc[tm][tn], 0, 0, 0); } }
        }
    }
    __device__ static __forceinline__ void run(const u16* A, int lda, const u16* B, int ldb, int k0, int nst, char* lds, f32x16 (&acc)[TM][TN]) {
        const int tid = threadIdx.x, w = __builtin_amdgcn_readfirstlane(tid >> 6), lane = tid & 63, wm = w / WN, wn = w % WN;
#pragma unroll
        for (int i = 0; i < TM; ++i)
#pragma unroll
            for (int j = 0; j < TN; ++j) acc[i][j] = f32x16{};
        stage(A, lda, B, ldb, k0, lds, tid);
        if (NSTG == 3 && nst > 1) stage(A, lda, B, ldb, k0 + 64, lds + STG, tid);
        int cb = 0;
        for (int t = 0; t < nst; ++t) {
            if (NSTG == 3) { if (t + 1 < nst) asm volatile("s_waitcnt vmcnt(%0)" :: "i"(NA + NB) : "memory"); else asm volatile("s_waitcnt vmcnt(0)" ::: "memory"); }
            else asm volatile("s_waitcnt vmcnt(0)" ::: "memory");
            __builtin_amdgcn_s_barrier();
            int nb = cb + NSTG - 1; if (nb >= NSTG) nb -= NSTG;
            if (t + NSTG - 1 < nst) stage(A, lda, B, ldb, k0 + (t + NSTG - 1) * 64, lds + nb * STG, tid);
            compute(lds + cb * STG, wm, wn, lane, acc);
            asm volatile("s_waitcnt lgkmcnt(0)" ::: "memory");
            if (++cb == NSTG) cb = 0;
        }
        __builtin_amdgcn_s_barrier();
    }
};

#define K1BN 128
constexpr int G1_BM = 128, G1_BN = K1BN, G1_WC = G1_BN / 4, G1_ST = G1_WC * 2 + 16;
constexpr int G1_LDS = 3 * (G1_BM + G1_BN) * 128;
__global__ __launch_bounds__(512, 1) void k1_proj(const u16* __restrict__ x, const u16* __restrict__ ckv, const u16* __restrict__ wq,
        const u16* __restrict__ wuk, const u16* __restrict__ wuv, const u16* __restrict__ w1, u16* __restrict__ qkv, u16* __restrict__ hbuf) {
    extern __shared__ __attribute__((aligned(16))) char lds[];
    using G = Gemm<G1_BM, G1_BN, 2, 4, true, 3>;
    constexpr int NT = NQ / G1_BN;
    int b = blockIdx.x; const int mt = b & 15; int nt = b >> 4;
    const u16* A; const u16* B; u16* C; int lda, ldb, ldc, nst; bool relu = false;
    if (nt < NT) { A = x; lda = DM; B = wq; ldb = NQ; C = qkv; ldc = NQ; nst = 9; }
    else if (nt < 2 * NT) { nt -= NT; A = ckv; lda = DC; B = wuk; ldb = NQ; C = qkv + (size_t)S_ * NQ; ldc = NQ; nst = 8; }
    else if (nt < 3 * NT) { nt -= 2 * NT; A = ckv; lda = DC; B = wuv; ldb = NQ; C = qkv + (size_t)2 * S_ * NQ; ldc = NQ; nst = 8; }
    else { nt -= 3 * NT; A = x; lda = DM; B = w1; ldb = DFF; C = hbuf; ldc = DFF; nst = 9; relu = true; }
    f32x16 acc[G::TM][G::TN];
    G::run(A + (size_t)mt * G1_BM * lda, lda, B + nt * G1_BN, ldb, 0, nst, lds, acc);
    const int tid = threadIdx.x, w = tid >> 6, lane = tid & 63, r32 = lane & 31, hi = lane >> 5, wm = w >> 2, wn = w & 3;
    char* ep = lds + w * (64 * G1_ST);
#pragma unroll
    for (int tm = 0; tm < G::TM; ++tm)
#pragma unroll
        for (int tn = 0; tn < G::TN; ++tn)
#pragma unroll
            for (int q = 0; q < 4; ++q) { f32x16& a = acc[tm][tn]; float v0 = a[4 * q], v1 = a[4 * q + 1], v2 = a[4 * q + 2], v3 = a[4 * q + 3];
                if (relu) { v0 = fmaxf(v0, 0.f); v1 = fmaxf(v1, 0.f); v2 = fmaxf(v2, 0.f); v3 = fmaxf(v3, 0.f); }
                u32x2 pk = {cvtpk(v0, v1), cvtpk(v2, v3)};
                *(u32x2*)(ep + (tm * 32 + r32) * G1_ST + (tn * 32 + 8 * q + 4 * hi) * 2) = pk; }
    asm volatile("s_waitcnt lgkmcnt(0)" ::: "memory");
    u16* Cw = C + (size_t)(mt * G1_BM + wm * 64) * ldc + nt * G1_BN + wn * G1_WC;
    constexpr int CPR = G1_WC / 8, RPI = 64 / CPR;
#pragma unroll
    for (int i = 0; i < 64 / RPI; ++i) { const int row = i * RPI + lane / CPR, ch = lane % CPR;
        const u32x4 v = *(const u32x4*)(ep + row * G1_ST + ch * 16);
        *(u32x4*)(Cw + (size_t)row * ldc + ch * 8) = v; }
}

constexpr int G3_LDS = 3 * (128 * 128 + 192 * 128);
__global__ __launch_bounds__(512, 1) void k3_out(const u16* __restrict__ ctx, const u16* __restrict__ wo, const u16* __restrict__ hbuf,
        const u16* __restrict__ w2, float* __restrict__ part) {
    extern __shared__ __attribute__((aligned(16))) char lds[];
    using G = Gemm<128, 192, 4, 2, false, 3>;
    const int b = blockIdx.x, tile = b % 48, sp = b / 48, mt = tile & 15, nt = tile >> 4;
    const u16* A; const u16* B; int lda, k0, nst;
    if (sp < 2) { A = ctx; lda = NQ; B = wo; k0 = sp * 2048; nst = 32; }
    else { A = hbuf; lda = DFF; B = w2; k0 = (sp - 2) * 1536; nst = 24; }
    f32x16 acc[G::TM][G::TN];
    G::run(A + (size_t)mt * 128 * lda, lda, B + nt * 192, DM, k0, nst, lds, acc);
    const int tid = threadIdx.x, w = tid >> 6, lane = tid & 63, r32 = lane & 31, hi = lane >> 5, wm = w >> 1, wn = w & 1;
    float* P = part + (size_t)sp * S_ * DM + (size_t)(mt * 128 + wm * 32) * DM + nt * 192 + wn * 96 + r32;
#pragma unroll
    for (int tn = 0; tn < 3; ++tn)
#pragma unroll
        for (int r = 0; r < 16; ++r) P[(size_t)crow(r, hi) * DM + tn * 32] = acc[0][tn][r];
}

__device__ __forceinline__ float bfr(float v) { return __uint_as_float(cvtpk(v, v) << 16); }
__device__ __forceinline__ float bf2f(u16 v) { return __uint_as_float((unsigned)v << 16); }
__global__ __launch_bounds__(256) void k4_final(const float* __restrict__ part, const u16* __restrict__ x, const u16* __restrict__ gw, u16* __restrict__ out) {
    const int wv = threadIdx.x >> 6, lane = threadIdx.x & 63, row = blockIdx.x * 4 + wv;
    float s = 0.f;
    for (int i = lane; i < DM; i += 64) s += bfr(bf2f(x[(size_t)row * DM + i]) * bf2f(gw[i]));
    for (int o = 32; o > 0; o >>= 1) s += __shfl_xor(s, o);
    s = bfr(s);
    const float g = bfr(1.f / (1.f + __expf(-s))), g1 = bfr(1.f - g);
    for (int c = lane * 4; c < DM; c += 256) {
        f32x4 a = {0, 0, 0, 0}, bsum = {0, 0, 0, 0};
#pragma unroll
        for (int p = 0; p < 5; ++p) { const f32x4 v = *(const f32x4*)(part + ((size_t)p * S_ + row) * DM + c); if (p < 2) a += v; else bsum += v; }
        float o4[4];
#pragma unroll
        for (int j = 0; j < 4; ++j) o4[j] = bfr(g * bfr(a[j])) + bfr(g1 * bfr(bsum[j]));
        u32x2 pk = {cvtpk(o4[0], o4[1]), cvtpk(o4[2], o4[3])};
        *(u32x2*)(out + (size_t)row * DM + c) = pk;
    }
}

constexpr float SCALE = 0.08838834764831845f;
constexpr int AT_BUF = 65536;
constexpr int AT_LDS = 2 * AT_BUF + 8 * 256;
__device__ __forceinline__ void stage_k(const u16* Kh, int key0, char* buf, int tid) {
    const int w = tid >> 6, lane = tid & 63;
    int ko = (w * 4 + (lane >> 4)) * NQ + (((lane & 15) ^ ((lane >> 4) & 3) ^ ((w & 1) << 2)) << 3);
    asm volatile("" : "+v"(ko));
    const u16* kp = Kh + (size_t)key0 * NQ;
#pragma unroll
    for (int i = 0; i < 4; ++i) glds16(kp + (size_t)i * 32 * NQ + ko, buf + (i * 8 + w) * 1024);
}
__device__ __forceinline__ void stage_v(const u16* Vh, int key0, char* buf, int tid) {
    const int w = tid >> 6, lane = tid & 63;
    const int kk = (w >> 1) * 8 + ((lane >> 2) & 7), k = (kk & ~0xC) | ((kk & 4) << 1) | ((kk & 8) >> 1);
    int vo = k * NQ + ((w & 1) * 2 + (lane >> 5)) * 32 + (lane & 3) * 8;
    asm volatile("" : "+v"(vo));
    const u16* vp = Vh + (size_t)key0 * NQ;
#pragma unroll
    for (int i = 0; i < 4; ++i) glds16(vp + (size_t)i * 32 * NQ + vo, buf + 32768 + (i * 8 + w) * 1024);
}
__global__ __launch_bounds__(512, 1) void k2_attn(const u16* __restrict__ qkv, u16* __restrict__ ctx) {
    extern __shared__ __attribute__((aligned(16))) char lds[];
    const int tid = threadIdx.x, w = __builtin_amdgcn_readfirstlane(tid >> 6), lane = tid & 63, r32 = lane & 31, hi = lane >> 5;
    const int grp = w >> 2, wq = w & 3;
    const int head = (blockIdx.x & 7) * 4 + (blockIdx.x >> 6), pr = (blockIdx.x >> 3) & 7;
    const u16* Qh = qkv + head * HD; const u16* Kh = qkv + (size_t)S_ * NQ + head * HD; const u16* Vh = qkv + (size_t)2 * S_ * NQ + head * HD;
    float* sm = (float*)(lds + 2 * AT_BUF) + w * 64;
    constexpr float C2 = 1.4426950408889634f * SCALE;
    for (int pass = 0; pass < 2; ++pass) {
        const int xb = pass == 0 ? 15 - pr : pr, q0 = xb * 128 + wq * 32, nit = xb + 1;
        bf16x8 qr[8];
#pragma unroll
        for (int d0 = 0; d0 < 8; ++d0) qr[d0] = *(const bf16x8*)(Qh + (size_t)(q0 + r32) * NQ + d0 * 16 + hi * 8);
        float m_reg = -1e30f, l_reg = 0.f; f32x16 o[4] = {};
        stage_k(Kh, 0, lds, tid);
        asm volatile("s_waitcnt vmcnt(0)" ::: "memory"); __builtin_amdgcn_s_barrier();
        f32x16 y0, y1; float alpha = 1.f;
        auto step = [&](int it, auto DOQ, auto DOP) __attribute__((always_inline)) {
            const char* cur = lds + (it & 1) * AT_BUF;
            f32x16 p0 = {}, p1 = {};
            if constexpr (decltype(DOQ)::value) {
                if (it + 1 < nit) stage_k(Kh, (it + 1) * 128, lds + ((it + 1) & 1) * AT_BUF, tid);
                stage_v(Vh, it * 128, lds + (it & 1) * AT_BUF, tid);
                const char* Kt = cur + grp * 16384;
#pragma unroll
                for (int dd = 0; dd < 4; ++dd) { const char* a = Kt + r32 * 256 + (((dd * 2 + hi) ^ (r32 & 7)) << 4);
#pragma unroll
                    for (int e = 0; e < 2; ++e) {
                    const bf16x8 b0 = *(const bf16x8*)(a + e * 128), b1 = *(const bf16x8*)(a + e * 128 + 8192);
                    p0 = __builtin_amdgcn_mfma_f32_32x32x16_bf16(b0, qr[dd + 4 * e], p0, 0, 0, 0);
                    p1 = __builtin_amdgcn_mfma_f32_32x32x16_bf16(b1, qr[dd + 4 * e], p1, 0, 0, 0); } __builtin_amdgcn_sched_group_barrier(0x100, 4, 0); }
            }
            if constexpr (decltype(DOP)::value) {
                float ps = 0.f;
#pragma unroll
                for (int r = 0; r < 16; ++r) { y0[r] = __builtin_amdgcn_exp2f(y0[r]); y1[r] = __builtin_amdgcn_exp2f(y1[r]); ps += y0[r] + y1[r]; }
                { auto rr = __builtin_amdgcn_permlane32_swap(__float_as_uint(ps), __float_as_uint(ps), false, false);
                  ps = __uint_as_float(rr[0]) + __uint_as_float(rr[1]); }
                l_reg = l_reg * alpha + ps;
                bf16x8 pa[4];
#define PK4(P, B_, OUT) do { unsigned a0 = cvtpk(P[B_+0], P[B_+1]), a1 = cvtpk(P[B_+2], P[B_+3]), b0 = cvtpk(P[B_+4], P[B_+5]), b1 = cvtpk(P[B_+6], P[B_+7]); \
        auto r0 = __builtin_amdgcn_permlane32_swap(a0, b0, false, false); auto r1 = __builtin_amdgcn_permlane32_swap(a1, b1, false, false); \
        u32x4 ww = {r0[0], r1[0], r0[1], r1[1]}; OUT = *reinterpret_cast<bf16x8*>(&ww); } while (0)
                PK4(y0, 0, pa[0]); PK4(y0, 8, pa[1]); PK4(y1, 0, pa[2]); PK4(y1, 8, pa[3]);
#undef PK4
                const int vb0 = (int)(uintptr_t)(lds + ((it - 1) & 1) * AT_BUF + 32768 + grp * 16384) + v_rd_base(lane);
#pragma unroll
                for (int dh = 0; dh < 8; ++dh) { const int d0 = dh >> 1, k2 = (dh & 1) * 2; s16x4 lo[4], hh[4];
#pragma unroll
                    for (int ks = k2; ks < k2 + 2; ++ks) {
                        asm volatile("ds_read_b64_tr_b16 %0, %1 offset:%2" : "=&v"(lo[ks]) : "v"(vb0), "i"(d0 * 512 + ks * 4096) : "memory");
                        asm volatile("ds_read_b64_tr_b16 %0, %1 offset:%2" : "=&v"(hh[ks]) : "v"(vb0), "i"(d0 * 512 + ks * 4096 + 2048) : "memory"); }
                    asm volatile("s_waitcnt lgkmcnt(0)" ::: "memory"); SBAR();
#pragma unroll
                    for (int ks = k2; ks < k2 + 2; ++ks) o[d0] = __builtin_amdgcn_mfma_f32_32x32x16_bf16(pa[ks],
                        (bf16x8){lo[ks][0], lo[ks][1], lo[ks][2], lo[ks][3], hh[ks][0], hh[ks][1], hh[ks][2], hh[ks][3]}, o[d0], 0, 0, 0); }
            }
            if constexpr (decltype(DOQ)::value) {
                const int kb = (2 * it + grp) * 64;
                if (kb + 63 > q0) { const int dq = q0 + r32 - 4 * hi - kb; const float NEG = -__builtin_inff();
#pragma unroll
                    for (int r = 0; r < 16; ++r) { const int c = (r & 3) + 8 * (r >> 2); if (dq - c < 0) p0[r] = NEG; if (dq - c - 32 < 0) p1[r] = NEG; } }
                float pmax = p0[0];
#pragma unroll
                for (int r = 1; r < 16; ++r) pmax = fmaxf(pmax, p0[r]);
#pragma unroll
                for (int r = 0; r < 16; ++r) pmax = fmaxf(pmax, p1[r]);
                { auto rr = __builtin_amdgcn_permlane32_swap(__float_as_uint(pmax), __float_as_uint(pmax), false, false);
                  pmax = fmaxf(__uint_as_float(rr[0]), __uint_as_float(rr[1])); }
                const float mn = fmaxf(m_reg, pmax); alpha = __builtin_amdgcn_exp2f((m_reg - mn) * C2); m_reg = mn;
                const float mnL = -mn * C2;
#pragma unroll
                for (int r = 0; r < 16; ++r) { y0[r] = fmaf(p0[r], C2, mnL); y1[r] = fmaf(p1[r], C2, mnL); }
                if (__any(alpha < 1.f)) { if (hi == 0) sm[r32] = alpha; asm volatile("s_waitcnt lgkmcnt(0)" ::: "memory");
#pragma unroll
                    for (int d_ = 0; d_ < 4; ++d_)
#pragma unroll
                        for (int r = 0; r < 16; ++r) o[d_][r] *= sm[crow(r, hi)]; }
            }
            asm volatile("s_waitcnt vmcnt(0) lgkmcnt(0)" ::: "memory"); __builtin_amdgcn_s_barrier();
        };
        step(0, std::true_type{}, std::false_type{});
        for (int it = 1; it < nit; ++it) step(it, std::true_type{}, std::true_type{});
        step(nit, std::false_type{}, std::true_type{});
        float* ob = (float*)lds + wq * 4096; float* ml = (float*)(lds + 2 * AT_BUF) + (wq + 4) * 64;
        if (grp == 1) { if (hi == 0) { ml[r32] = m_reg; ml[32 + r32] = l_reg; }
#pragma unroll
            for (int d0 = 0; d0 < 4; ++d0)
#pragma unroll
                for (int r = 0; r < 16; ++r) ob[crow(r, hi) * 128 + d0 * 32 + r32] = o[d0][r]; }
        asm volatile("s_waitcnt lgkmcnt(0)" ::: "memory"); __builtin_amdgcn_s_barrier();
        if (grp == 0) {
            const float m1 = ml[r32], l1 = ml[32 + r32], mn = fmaxf(m_reg, m1);
            const float a0 = __builtin_amdgcn_exp2f((m_reg - mn) * C2), a1 = __builtin_amdgcn_exp2f((m1 - mn) * C2), rl = 1.f / (l_reg * a0 + l1 * a1);
            if (hi == 0) { sm[r32] = a0 * rl; sm[32 + r32] = a1 * rl; } asm volatile("s_waitcnt lgkmcnt(0)" ::: "memory");
            u16* Ow = ctx + (size_t)q0 * NQ + head * HD;
#pragma unroll
            for (int r = 0; r < 16; ++r) { const int orow = crow(r, hi); const float f0 = sm[orow], f1 = sm[32 + orow];
#pragma unroll
                for (int d0 = 0; d0 < 4; ++d0) { const float v = o[d0][r] * f0 + ob[orow * 128 + d0 * 32 + r32] * f1; const float vn = __shfl_xor(v, 1);
                    if ((r32 & 1) == 0) *(unsigned*)(Ow + (size_t)orow * NQ + d0 * 32 + r32) = cvtpk(v, vn); } }
        }
        __syncthreads();
    }
}

extern "C" void kernel_launch(void* const* d_in, const int* in_sizes, int n_in, void* d_out, int out_size, void* d_ws, size_t ws_size, hipStream_t stream) {
    const u16* x = (const u16*)d_in[0]; const u16* ckv = (const u16*)d_in[1]; const u16* wq = (const u16*)d_in[2]; const u16* wuk = (const u16*)d_in[3];
    const u16* wuv = (const u16*)d_in[4]; const u16* wo = (const u16*)d_in[5]; const u16* w1 = (const u16*)d_in[7]; const u16* w2 = (const u16*)d_in[8];
    const u16* gw = (const u16*)d_in[9];
    char* ws = (char*)d_ws;
    u16* qkv = (u16*)ws;
    u16* hbuf = (u16*)(ws + (size_t)48 * 1048576);
    u16* ctx = (u16*)(ws + (size_t)68 * 1048576);
    float* part = (float*)(ws + (size_t)84 * 1048576);
    hipFuncSetAttribute((const void*)k1_proj, hipFuncAttributeMaxDynamicSharedMemorySize, G1_LDS);
    hipFuncSetAttribute((const void*)k2_attn, hipFuncAttributeMaxDynamicSharedMemorySize, AT_LDS);
    hipFuncSetAttribute((const void*)k3_out, hipFuncAttributeMaxDynamicSharedMemorySize, G3_LDS);
    k1_proj<<<16 * (3 * NQ + DFF) / G1_BN, 512, G1_LDS, stream>>>(x, ckv, wq, wuk, wuv, w1, qkv, hbuf);
    k2_attn<<<256, 512, AT_LDS, stream>>>(qkv, ctx);
    k3_out<<<240, 512, G3_LDS, stream>>>(ctx, wo, hbuf, w2, part);
    k4_final<<<512, 256, 0, stream>>>(part, x, gw, (u16*)d_out);
}
```

```cpp
#include <hip/hip_runtime.h>
#include <hip/hip_bf16.h>
#include <cstdint>
#include <type_traits>

typedef short bf16x8 __attribute__((ext_vector_type(8)));
typedef short s16x4 __attribute__((ext_vector_type(4)));
typedef float f32x16 __attribute__((ext_vector_type(16)));
typedef float f32x4 __attribute__((ext_vector_type(4)));
typedef unsigned u32x4 __attribute__((ext_vector_type(4)));
typedef unsigned u32x2 __attribute__((ext_vector_type(2)));
typedef unsigned short u16;

constexpr int S_ = 2048, DM = 576, DC = 512, NH = 32, HD = 128, NQ = 4096, DFF = 4608;

__device__ __forceinline__ unsigned cvtpk(float lo, float hi) {
    unsigned r; asm volatile("v_cvt_pk_bf16_f32 %0, %1, %2" : "=v"(r) : "v"(lo), "v"(hi)); return r;
}
__device__ __forceinline__ int crow(int r, int hi) { return (r & 3) + 8 * (r >> 2) + 4 * hi; }
__device__ __forceinline__ int v_rd_base(int lane) { return ((lane & 3) << 3) | (((lane >> 2) & 3) << 6) | (((lane >> 4) & 1) << 5) | (((lane >> 5) & 1) << 8); }
#define SBAR() __builtin_amdgcn_sched_barrier(0)

__device__ __forceinline__ void glds16(const void* g, void* l) {
    __builtin_amdgcn_global_load_lds((const __attribute__((address_space(1))) void*)g, (__attribute__((address_space(3))) void*)l, 16, 0, 0);
}

template <int BM, int BN, int WM, int WN, bool SWAP, int NSTG>
struct Gemm {
    static constexpr int BK = 64;
    static constexpr int TM = BM / WM / 32, TN = BN / WN / 32;
    static constexpr int A_BYTES = BM * BK * 2, B_BYTES = BN * BK * 2, STG = A_BYTES + B_BYTES;
    static constexpr int NA = BM * 8 / 512, NB = BN * 8 / 512, CB = BN / 32;
    __device__ static __forceinline__ void stage(const u16* A, int lda, const u16* B, int ldb, int k0, char* buf, int tid) {
        const int w = tid >> 6, lane = tid & 63;
#pragma unroll
        for (int i = 0; i < NA; ++i) { const int c = (i * 8 + w) * 64 + lane, row = c >> 3, sl = c & 7, sc = sl ^ ((row >> 1) & 7);
            glds16(A + (size_t)row * lda + k0 + sc * 8, buf + (i * 8 + w) * 1024); }
#pragma unroll
        for (int i = 0; i < NB; ++i) { const int c = (i * 8 + w) * 64 + lane, s = c >> 5, rk = (c >> 2) & 7, cc = c & 3;
            const int kh = s / CB, cb = s - kh * CB, kk = kh * 8 + rk, k = (kk & ~0xC) | ((kk & 4) << 1) | ((kk & 8) >> 1);
            glds16(B + (size_t)(k0 + k) * ldb + cb * 32 + cc * 8, buf + A_BYTES + (i * 8 + w) * 1024); }
    }
    __device__ static __forceinline__ void compute(const char* buf, int wm, int wn, int lane, f32x16 (&acc)[TM][TN]) {
        const int r32 = lane & 31, hi = lane >> 5;
        const int bb = (int)(uintptr_t)(buf + A_BYTES) + v_rd_base(lane) + wn * TN * 512;
        int ab[4];
        { const int row = wm * TM * 32 + r32; const int sw = (row >> 1) & 7;
          _Pragma("unroll") for (int q = 0; q < 4; ++q) ab[q] = (int)(uintptr_t)buf + row * 128 + (((2 * q + hi) ^ sw) << 4); }
        bf16x8 af[2][TM]; s16x4 bl[2][TN], bh[2][TN];
#define RD(ks, S) do { _Pragma("unroll") for (int t = 0; t < TN; ++t) { \
            asm volatile("ds_read_b64_tr_b16 %0, %1 offset:%2" : "=&v"(bl[S][t]) : "v"(bb), "i"(((ks) * 2 * CB + t) * 512) : "memory"); \
            asm volatile("ds_read_b64_tr_b16 %0, %1 offset:%2" : "=&v"(bh[S][t]) : "v"(bb), "i"((((ks) * 2 + 1) * CB + t) * 512) : "memory"); } \
        _Pragma("unroll") for (int t = 0; t < TM; ++t) asm volatile("ds_read_b128 %0, %1 offset:%2" : "=&v"(af[S][t]) : "v"(ab[ks]), "i"(t * 4096) : "memory"); } while (0)
        RD(0, 0);
#pragma unroll
        for (int ks = 0; ks < 4; ++ks) {
            if (ks < 3) { RD(ks + 1, (ks + 1) & 1); asm volatile("s_waitcnt lgkmcnt(%0)" :: "i"(TM + 2 * TN) : "memory"); }
            else asm volatile("s_waitcnt lgkmcnt(0)" ::: "memory");
            SBAR();
            const int S = ks & 1;
#pragma unroll
            for (int tn = 0; tn < TN; ++tn) { const bf16x8 bf = {bl[S][tn][0], bl[S][tn][1], bl[S][tn][2], bl[S][tn][3], bh[S][tn][0], bh[S][tn][1], bh[S][tn][2], bh[S][tn][3]};
#pragma unroll
                for (int tm = 0; tm < TM; ++tm) {
                    if (SWAP) acc[tm][tn] = __builtin_amdgcn_mfma_f32_32x32x16_bf16(bf, af[S][tm], acc[tm][tn], 0, 0, 0);
                    else      acc[tm][tn] = __builtin_amdgcn_mfma_f32_32x32x16_bf16(af[S][tm], bf, acc[tm][tn], 0, 0, 0); } }
            SBAR();
        }
#undef RD
    }
    __device__ static __forceinline__ void run(const u16* A, int lda, const u16* B, int ldb, int k0, int nst, char* lds, f32x16 (&acc)[TM][TN]) {
        const int tid = threadIdx.x, w = __builtin_amdgcn_readfirstlane(tid >> 6), lane = tid & 63, wm = w / WN, wn = w % WN;
#pragma unroll
        for (int i = 0; i < TM; ++i)
#pragma unroll
            for (int j = 0; j < TN; ++j) acc[i][j] = f32x16{};
        stage(A, lda, B, ldb, k0, lds, tid);
        if (NSTG == 3 && nst > 1) stage(A, lda, B, ldb, k0 + 64, lds + STG, tid);
        int cb = 0;
        for (int t = 0; t < nst; ++t) {
            if (NSTG == 3) { if (t + 1 < nst) asm volatile("s_waitcnt vmcnt(%0)" :: "i"(NA + NB) : "memory"); else asm volatile("s_waitcnt vmcnt(0)" ::: "memory"); }
            else asm volatile("s_waitcnt vmcnt(0)" ::: "memory");
            __builtin_amdgcn_s_barrier();
            int nb = cb + NSTG - 1; if (nb >= NSTG) nb -= NSTG;
            if (t + NSTG - 1 < nst) stage(A, lda, B, ldb, k0 + (t + NSTG - 1) * 64, lds + nb * STG, tid);
            compute(lds + cb * STG, wm, wn, lane, acc);
            asm volatile("s_waitcnt lgkmcnt(0)" ::: "memory");
            if (++cb == NSTG) cb = 0;
        }
        __builtin_amdgcn_s_barrier();
    }
};

#define K1BN 128
constexpr int G1_BM = 128, G1_BN = K1BN, G1_WC = G1_BN / 4, G1_ST = G1_WC * 2 + 16;
constexpr int G1_LDS = 2 * (G1_BM + G1_BN) * 128;
__global__ __launch_bounds__(512, 1) void k1_proj(const u16* __restrict__ x, const u16* __restrict__ ckv, const u16* __restrict__ wq,
        const u16* __restrict__ wuk, const u16* __restrict__ wuv, const u16* __restrict__ w1, u16* __restrict__ qkv, u16* __restrict__ hbuf) {
    extern __shared__ __attribute__((aligned(16))) char lds[];
    using G = Gemm<G1_BM, G1_BN, 2, 4, true, 2>;
    constexpr int NT = NQ / G1_BN;
    int b = blockIdx.x; const int mt = b & 15; int nt = b >> 4;
    const u16* A; const u16* B; u16* C; int lda, ldb, ldc, nst; bool relu = false;
    if (nt < NT) { A = x; lda = DM; B = wq; ldb = NQ; C = qkv; ldc = NQ; nst = 9; }
    else if (nt < 2 * NT) { nt -= NT; A = ckv; lda = DC; B = wuk; ldb = NQ; C = qkv + (size_t)S_ * NQ; ldc = NQ; nst = 8; }
    else if (nt < 3 * NT) { nt -= 2 * NT; A = ckv; lda = DC; B = wuv; ldb = NQ; C = qkv + (size_t)2 * S_ * NQ; ldc = NQ; nst = 8; }
    else { nt -= 3 * NT; A = x; lda = DM; B = w1; ldb = DFF; C = hbuf; ldc = DFF; nst = 9; relu = true; }
    f32x16 acc[G::TM][G::TN];
    G::run(A + (size_t)mt * G1_BM * lda, lda, B + nt * G1_BN, ldb, 0, nst, lds, acc);
    const int tid = threadIdx.x, w = tid >> 6, lane = tid & 63, r32 = lane & 31, hi = lane >> 5, wm = w >> 2, wn = w & 3;
    char* ep = lds + w * (64 * G1_ST);
#pragma unroll
    for (int tm = 0; tm < G::TM; ++tm)
#pragma unroll
        for (int tn = 0; tn < G::TN; ++tn)
#pragma unroll
            for (int q = 0; q < 4; ++q) { f32x16& a = acc[tm][tn]; float v0 = a[4 * q], v1 = a[4 * q + 1], v2 = a[4 * q + 2], v3 = a[4 * q + 3];
                if (relu) { v0 = fmaxf(v0, 0.f); v1 = fmaxf(v1, 0.f); v2 = fmaxf(v2, 0.f); v3 = fmaxf(v3, 0.f); }
                u32x2 pk = {cvtpk(v0, v1), cvtpk(v2, v3)};
                *(u32x2*)(ep + (tm * 32 + r32) * G1_ST + (tn * 32 + 8 * q + 4 * hi) * 2) = pk; }
    asm volatile("s_waitcnt lgkmcnt(0)" ::: "memory");
    u16* Cw = C + (size_t)(mt * G1_BM + wm * 64) * ldc + nt * G1_BN + wn * G1_WC;
    constexpr int CPR = G1_WC / 8, RPI = 64 / CPR;
#pragma unroll
    for (int i = 0; i < 64 / RPI; ++i) { const int row = i * RPI + lane / CPR, ch = lane % CPR;
        const u32x4 v = *(const u32x4*)(ep + row * G1_ST + ch * 16);
        *(u32x4*)(Cw + (size_t)row * ldc + ch * 8) = v; }
}

constexpr int G3_LDS = 3 * (128 * 128 + 192 * 128);
__global__ __launch_bounds__(512, 1) void k3_out(const u16* __restrict__ ctx, const u16* __restrict__ wo, const u16* __restrict__ hbuf,
        const u16* __restrict__ w2, float* __restrict__ part) {
    extern __shared__ __attribute__((aligned(16))) char lds[];
    using G = Gemm<128, 192, 4, 2, false, 3>;
    const int b = blockIdx.x, tile = b % 48, sp = b / 48, mt = tile & 15, nt = tile >> 4;
    const u16* A; const u16* B; int lda, k0, nst;
    if (sp < 2) { A = ctx; lda = NQ; B = wo; k0 = sp * 2048; nst = 32; }
    else { A = hbuf; lda = DFF; B = w2; k0 = (sp - 2) * 1536; nst = 24; }
    f32x16 acc[G::TM][G::TN];
    G::run(A + (size_t)mt * 128 * lda, lda, B + nt * 192, DM, k0, nst, lds, acc);
    const int tid = threadIdx.x, w = tid >> 6, lane = tid & 63, r32 = lane & 31, hi = lane >> 5, wm = w >> 1, wn = w & 1;
    float* P = part + (size_t)sp * S_ * DM + (size_t)(mt * 128 + wm * 32) * DM + nt * 192 + wn * 96 + r32;
#pragma unroll
    for (int tn = 0; tn < 3; ++tn)
#pragma unroll
        for (int r = 0; r < 16; ++r) P[(size_t)crow(r, hi) * DM + tn * 32] = acc[0][tn][r];
}

__device__ __forceinline__ float bfr(float v) { return __uint_as_float(cvtpk(v, v) << 16); }
__device__ __forceinline__ float bf2f(u16 v) { return __uint_as_float((unsigned)v << 16); }
__global__ __launch_bounds__(256) void k4_final(const float* __restrict__ part, const u16* __restrict__ x, const u16* __restrict__ gw, u16* __restrict__ out) {
    const int wv = threadIdx.x >> 6, lane = threadIdx.x & 63, row = blockIdx.x * 4 + wv;
    float s = 0.f;
    for (int i = lane; i < DM; i += 64) s += bfr(bf2f(x[(size_t)row * DM + i]) * bf2f(gw[i]));
    for (int o = 32; o > 0; o >>= 1) s += __shfl_xor(s, o);
    s = bfr(s);
    const float g = bfr(1.f / (1.f + __expf(-s))), g1 = bfr(1.f - g);
    for (int c = lane * 4; c < DM; c += 256) {
        f32x4 a = {0, 0, 0, 0}, bsum = {0, 0, 0, 0};
#pragma unroll
        for (int p = 0; p < 5; ++p) { const f32x4 v = *(const f32x4*)(part + ((size_t)p * S_ + row) * DM + c); if (p < 2) a += v; else bsum += v; }
        float o4[4];
#pragma unroll
        for (int j = 0; j < 4; ++j) o4[j] = bfr(g * bfr(a[j])) + bfr(g1 * bfr(bsum[j]));
        u32x2 pk = {cvtpk(o4[0], o4[1]), cvtpk(o4[2], o4[3])};
        *(u32x2*)(out + (size_t)row * DM + c) = pk;
    }
}

constexpr float SCALE = 0.08838834764831845f;
constexpr int AT_BUF = 65536;
constexpr int AT_LDS = 2 * AT_BUF + 8 * 256;
__device__ __forceinline__ void stage_k(const u16* Kh, int key0, char* buf, int tid) {
    const int w = tid >> 6, lane = tid & 63;
    int ko = (w * 4 + (lane >> 4)) * NQ + (((lane & 15) ^ ((lane >> 4) & 3) ^ ((w & 1) << 2)) << 3);
    asm volatile("" : "+v"(ko));
    const u16* kp = Kh + (size_t)key0 * NQ;
#pragma unroll
    for (int i = 0; i < 4; ++i) glds16(kp + (size_t)i * 32 * NQ + ko, buf + (i * 8 + w) * 1024);
}
__device__ __forceinline__ void stage_v(const u16* Vh, int key0, char* buf, int tid) {
    const int w = tid >> 6, lane = tid & 63;
    const int kk = (w >> 1) * 8 + ((lane >> 2) & 7), k = (kk & ~0xC) | ((kk & 4) << 1) | ((kk & 8) >> 1);
    int vo = k * NQ + ((w & 1) * 2 + (lane >> 5)) * 32 + (lane & 3) * 8;
    asm volatile("" : "+v"(vo));
    const u16* vp = Vh + (size_t)key0 * NQ;
#pragma unroll
    for (int i = 0; i < 4; ++i) glds16(vp + (size_t)i * 32 * NQ + vo, buf + 32768 + (i * 8 + w) * 1024);
}
__global__ __launch_bounds__(512, 1) void k2_attn(const u16* __restrict__ qkv, u16* __restrict__ ctx) {
    extern __shared__ __attribute__((aligned(16))) char lds[];
    const int tid = threadIdx.x, w = __builtin_amdgcn_readfirstlane(tid >> 6), lane = tid & 63, r32 = lane & 31, hi = lane >> 5;
    const int grp = w >> 2, wq = w & 3;
    const int head = (blockIdx.x & 7) * 4 + (blockIdx.x >> 6), pr = (blockIdx.x >> 3) & 7;
    const u16* Qh = qkv + head * HD; const u16* Kh = qkv + (size_t)S_ * NQ + head * HD; const u16* Vh = qkv + (size_t)2 * S_ * NQ + head * HD;
    float* sm = (float*)(lds + 2 * AT_BUF) + w * 64;
    constexpr float C2 = 1.4426950408889634f * SCALE;
    for (int pass = 0; pass < 2; ++pass) {
        const int xb = pass == 0 ? 15 - pr : pr, q0 = xb * 128 + wq * 32, nit = xb + 1;
        bf16x8 qr[8];
#pragma unroll
        for (int d0 = 0; d0 < 8; ++d0) qr[d0] = *(const bf16x8*)(Qh + (size_t)(q0 + r32) * NQ + d0 * 16 + hi * 8);
        float m_reg = -1e30f, l_reg = 0.f; f32x16 o[4] = {};
        stage_k(Kh, 0, lds, tid);
        asm volatile("s_waitcnt vmcnt(0)" ::: "memory"); __builtin_amdgcn_s_barrier();
        f32x16 y0, y1; float alpha = 1.f;
        auto step = [&](int it, auto DOQ, auto DOP) __attribute__((always_inline)) {
            const char* cur = lds + (it & 1) * AT_BUF;
            f32x16 p0 = {}, p1 = {};
            if constexpr (decltype(DOQ)::value) {
                if (it + 1 < nit) stage_k(Kh, (it + 1) * 128, lds + ((it + 1) & 1) * AT_BUF, tid);
                stage_v(Vh, it * 128, lds + (it & 1) * AT_BUF, tid);
                const char* Kt = cur + grp * 16384;
#pragma unroll
                for (int dd = 0; dd < 4; ++dd) { const char* a = Kt + r32 * 256 + (((dd * 2 + hi) ^ (r32 & 7)) << 4);
#pragma unroll
                    for (int e = 0; e < 2; ++e) {
                    const bf16x8 b0 = *(const bf16x8*)(a + e * 128), b1 = *(const bf16x8*)(a + e * 128 + 8192);
                    p0 = __builtin_amdgcn_mfma_f32_32x32x16_bf16(b0, qr[dd + 4 * e], p0, 0, 0, 0);
                    p1 = __builtin_amdgcn_mfma_f32_32x32x16_bf16(b1, qr[dd + 4 * e], p1, 0, 0, 0); } __builtin_amdgcn_sched_group_barrier(0x100, 4, 0); }
            }
            if constexpr (decltype(DOP)::value) {
                float ps = 0.f;
#pragma unroll
                for (int r = 0; r < 16; ++r) { y0[r] = __builtin_amdgcn_exp2f(y0[r]); y1[r] = __builtin_amdgcn_exp2f(y1[r]); ps += y0[r] + y1[r]; }
                { auto rr = __builtin_amdgcn_permlane32_swap(__float_as_uint(ps), __float_as_uint(ps), false, false);
                  ps = __uint_as_float(rr[0]) + __uint_as_float(rr[1]); }
                l_reg = l_reg * alpha + ps;
                bf16x8 pa[4];
#define PK4(P, B_, OUT) do { unsigned a0 = cvtpk(P[B_+0], P[B_+1]), a1 = cvtpk(P[B_+2], P[B_+3]), b0 = cvtpk(P[B_+4], P[B_+5]), b1 = cvtpk(P[B_+6], P[B_+7]); \
        auto r0 = __builtin_amdgcn_permlane32_swap(a0, b0, false, false); auto r1 = __builtin_amdgcn_permlane32_swap(a1, b1, false, false); \
        u32x4 ww = {r0[0], r1[0], r0[1], r1[1]}; OUT = *reinterpret_cast<bf16x8*>(&ww); } while (0)
                PK4(y0, 0, pa[0]); PK4(y0, 8, pa[1]); PK4(y1, 0, pa[2]); PK4(y1, 8, pa[3]);
#undef PK4
                const int vb0 = (int)(uintptr_t)(lds + ((it - 1) & 1) * AT_BUF + 32768 + grp * 16384) + v_rd_base(lane);
#pragma unroll
                for (int dh = 0; dh < 8; ++dh) { const int d0 = dh >> 1, k2 = (dh & 1) * 2; s16x4 lo[4], hh[4];
#pragma unroll
                    for (int ks = k2; ks < k2 + 2; ++ks) {
                        asm volatile("ds_read_b64_tr_b16 %0, %1 offset:%2" : "=&v"(lo[ks]) : "v"(vb0), "i"(d0 * 512 + ks * 4096) : "memory");
                        asm volatile("ds_read_b64_tr_b16 %0, %1 offset:%2" : "=&v"(hh[ks]) : "v"(vb0), "i"(d0 * 512 + ks * 4096 + 2048) : "memory"); }
                    asm volatile("s_waitcnt lgkmcnt(0)" ::: "memory"); SBAR();
#pragma unroll
                    for (int ks = k2; ks < k2 + 2; ++ks) o[d0] = __builtin_amdgcn_mfma_f32_32x32x16_bf16(pa[ks],
                        (bf16x8){lo[ks][0], lo[ks][1], lo[ks][2], lo[ks][3], hh[ks][0], hh[ks][1], hh[ks][2], hh[ks][3]}, o[d0], 0, 0, 0); }
            }
            if constexpr (decltype(DOQ)::value) {
                const int kb = (2 * it + grp) * 64;
                if (kb + 63 > q0) { const int dq = q0 + r32 - 4 * hi - kb; const float NEG = -__builtin_inff();
#pragma unroll
                    for (int r = 0; r < 16; ++r) { const int c = (r & 3) + 8 * (r >> 2); if (dq - c < 0) p0[r] = NEG; if (dq - c - 32 < 0) p1[r] = NEG; } }
                float pmax = p0[0];
#pragma unroll
                for (int r = 1; r < 16; ++r) pmax = fmaxf(pmax, p0[r]);
#pragma unroll
                for (int r = 0; r < 16; ++r) pmax = fmaxf(pmax, p1[r]);
                { auto rr = __builtin_amdgcn_permlane32_swap(__float_as_uint(pmax), __float_as_uint(pmax), false, false);
                  pmax = fmaxf(__uint_as_float(rr[0]), __uint_as_float(rr[1])); }
                const float mn = fmaxf(m_reg, pmax); alpha = __builtin_amdgcn_exp2f((m_reg - mn) * C2); m_reg = mn;
                const float mnL = -mn * C2;
#pragma unroll
                for (int r = 0; r < 16; ++r) { y0[r] = fmaf(p0[r], C2, mnL); y1[r] = fmaf(p1[r], C2, mnL); }
                if (__any(alpha < 1.f)) { if (hi == 0) sm[r32] = alpha; asm volatile("s_waitcnt lgkmcnt(0)" ::: "memory");
#pragma unroll
                    for (int d_ = 0; d_ < 4; ++d_)
#pragma unroll
                        for (int r = 0; r < 16; ++r) o[d_][r] *= sm[crow(r, hi)]; }
            }
            asm volatile("s_waitcnt vmcnt(0) lgkmcnt(0)" ::: "memory"); __builtin_amdgcn_s_barrier();
        };
        step(0, std::true_type{}, std::false_type{});
        for (int it = 1; it < nit; ++it) step(it, std::true_type{}, std::true_type{});
        step(nit, std::false_type{}, std::true_type{});
        float* ob = (float*)lds + wq * 4096; float* ml = (float*)(lds + 2 * AT_BUF) + (wq + 4) * 64;
        if (grp == 1) { if (hi == 0) { ml[r32] = m_reg; ml[32 + r32] = l_reg; }
#pragma unroll
            for (int d0 = 0; d0 < 4; ++d0)
#pragma unroll
                for (int r = 0; r < 16; ++r) ob[crow(r, hi) * 128 + d0 * 32 + r32] = o[d0][r]; }
        asm volatile("s_waitcnt lgkmcnt(0)" ::: "memory"); __builtin_amdgcn_s_barrier();
        if (grp == 0) {
            const float m1 = ml[r32], l1 = ml[32 + r32], mn = fmaxf(m_reg, m1);
            const float a0 = __builtin_amdgcn_exp2f((m_reg - mn) * C2), a1 = __builtin_amdgcn_exp2f((m1 - mn) * C2), rl = 1.f / (l_reg * a0 + l1 * a1);
            if (hi == 0) { sm[r32] = a0 * rl; sm[32 + r32] = a1 * rl; } asm volatile("s_waitcnt lgkmcnt(0)" ::: "memory");
            u16* Ow = ctx + (size_t)q0 * NQ + head * HD;
#pragma unroll
            for (int r = 0; r < 16; ++r) { const int orow = crow(r, hi); const float f0 = sm[orow], f1 = sm[32 + orow];
#pragma unroll
                for (int d0 = 0; d0 < 4; ++d0) { const float v = o[d0][r] * f0 + ob[orow * 128 + d0 * 32 + r32] * f1; const float vn = __shfl_xor(v, 1);
                    if ((r32 & 1) == 0) *(unsigned*)(Ow + (size_t)orow * NQ + d0 * 32 + r32) = cvtpk(v, vn); } }
        }
        __syncthreads();
    }
}

extern "C" void kernel_launch(void* const* d_in, const int* in_sizes, int n_in, void* d_out, int out_size, void* d_ws, size_t ws_size, hipStream_t stream) {
    const u16* x = (const u16*)d_in[0]; const u16* ckv = (const u16*)d_in[1]; const u16* wq = (const u16*)d_in[2]; const u16* wuk = (const u16*)d_in[3];
    const u16* wuv = (const u16*)d_in[4]; const u16* wo = (const u16*)d_in[5]; const u16* w1 = (const u16*)d_in[7]; const u16* w2 = (const u16*)d_in[8];
    const u16* gw = (const u16*)d_in[9];
    char* ws = (char*)d_ws;
    u16* qkv = (u16*)ws;
    u16* hbuf = (u16*)(ws + (size_t)48 * 1048576);
    u16* ctx = (u16*)(ws + (size_t)68 * 1048576);
    float* part = (float*)(ws + (size_t)84 * 1048576);
    hipFuncSetAttribute((const void*)k1_proj, hipFuncAttributeMaxDynamicSharedMemorySize, G1_LDS);
    hipFuncSetAttribute((const void*)k2_attn, hipFuncAttributeMaxDynamicSharedMemorySize, AT_LDS);
    hipFuncSetAttribute((const void*)k3_out, hipFuncAttributeMaxDynamicSharedMemorySize, G3_LDS);
    k1_proj<<<16 * (3 * NQ + DFF) / G1_BN, 512, G1_LDS, stream>>>(x, ckv, wq, wuk, wuv, w1, qkv, hbuf);
    k2_attn<<<256, 512, AT_LDS, stream>>>(qkv, ctx);
    k3_out<<<240, 512, G3_LDS, stream>>>(ctx, wo, hbuf, w2, part);
    k4_final<<<512, 256, 0, stream>>>(part, x, gw, (u16*)d_out);
}
```
